```python
import torch

def _softmax(x, axis=-1):
    return torch.softmax(x, dim=axis)
def _gelu(x):
    return torch.nn.functional.gelu(x, approximate="tanh")
def _silu(x):
    return torch.nn.functional.silu(x)
def _sigmoid(x):
    return torch.sigmoid(x)
def _layernorm(x, axis=-1, eps=1e-5):
    m = x.mean(dim=axis, keepdim=True)
    v = x.var(dim=axis, keepdim=True, unbiased=False)
    return (x - m) / torch.sqrt(v + eps)
def _scatter_add(tgt, idx, src, axis=0):
    return tgt.clone().index_add_(axis, idx, src)
def _segment_sum(src, seg, n):
    out = torch.zeros((n,) + src.shape[1:], dtype=src.dtype)
    return out.index_add_(0, seg, src)
def _pad(x, pw):
    flat = tuple(v for pair in reversed(pw) for v in pair)
    return torch.nn.functional.pad(x, flat)

kernel_name = 'sequential_preLN__sliding_local_dilated__mixer_token_mlp__prompt'

def setup_inputs():

    _w = (lambda a: (lambda t: t.to(torch.bfloat16) if t.is_floating_point() else t)(torch.as_tensor(a)))
    def _A_setup():
        B, S, D = 1, 2048, 640
        return {
            "x": _w((lambda *s: torch.randn(*s, dtype=torch.bfloat16) * 0.05)(B, S, D)),
            "wqkv": _w((lambda *s: torch.randn(*s, dtype=torch.bfloat16) * 0.05)(D, 3 * D)),
            "wo": _w((lambda *s: torch.randn(*s, dtype=torch.bfloat16) * 0.05)(D, D)),
        }
    _A_in = _A_setup()

    def _B_setup():
        B, S, D, F = 1, 2048, 640, 1536
        return {
            "x": _w((lambda *s: torch.randn(*s, dtype=torch.bfloat16) * 0.05)(B, S, D)),
            "w1": _w((lambda *s: torch.randn(*s, dtype=torch.bfloat16) * 0.8)(S, F) * (S ** -0.5)),
            "b1": _w((lambda *s: torch.randn(*s, dtype=torch.bfloat16) * 0.8)(F) * 0.02),
            "w2": _w((lambda *s: torch.randn(*s, dtype=torch.bfloat16) * 0.8)(F, S) * (F ** -0.5)),
            "b2": _w((lambda *s: torch.randn(*s, dtype=torch.bfloat16) * 0.8)(S) * 0.02),
        }
    _B_in = _B_setup()

    return {
        "A_x": _A_in["x"],
        "A_wqkv": _A_in["wqkv"],
        "A_wo": _A_in["wo"],
        "B_x": _B_in["x"],
        "B_w1": _B_in["w1"],
        "B_b1": _B_in["b1"],
        "B_w2": _B_in["w2"],
        "B_b2": _B_in["b2"],
    }

def reference(A_x, A_wqkv, A_wo, B_x, B_w1, B_b1, B_w2, B_b2):
    def _A_ref(x, wqkv, wo):
        B, S, D = x.shape
        H = 32; Dh = D // H
        W, dil = 8, 2
        qkv = (lambda x, *d: x.permute(*d))((x @ wqkv).reshape(B, S, 3, H, Dh), 2, 0, 3, 1, 4)
        q, k, v = qkv[0], qkv[1], qkv[2]
        scale = Dh ** -0.5
        pos = torch.arange(S)
        off = torch.arange(W) * dil
        idx = pos[:, None] - off[None, :]
        mask = idx >= 0
        idx_c = torch.where(mask, idx, 0).reshape(-1)
        kg = k[:, :, idx_c, :].reshape(B, H, S, W, Dh)
        vg = v[:, :, idx_c, :].reshape(B, H, S, W, Dh)
        logits = torch.einsum("bhsd,bhswd->bhsw", q, kg) * scale
        logits = torch.where(mask[None, None, :, :], logits, -1e9)
        attn = _softmax(logits, axis=-1)
        o = torch.einsum("bhsw,bhswd->bhsd", attn, vg)
        return (lambda x, *d: x.permute(*d))(o, 0, 2, 1, 3).reshape(B, S, D) @ wo

    def _B_ref(x, w1, b1, w2, b2):
        B, S, D = x.shape
        h = _layernorm(x, axis=-1)
        u = torch.einsum("bsd,sf->bdf", h, w1) + b1
        u = _gelu(u)
        y = torch.einsum("bdf,fs->bds", u, w2) + b2
        return (lambda x, *d: x.permute(*d))(y, 0, 2, 1)

    x = A_x
    h = x + _A_ref(_layernorm(x, axis=-1), A_wqkv, A_wo)
    h2 = h + _B_ref(_layernorm(h, axis=-1), B_w1, B_b1, B_w2, B_b2)
    return h2
```

```cpp
#include <hip/hip_runtime.h>
#include <hip/hip_bf16.h>

typedef unsigned v4u __attribute__((ext_vector_type(4)));
typedef unsigned v2u __attribute__((ext_vector_type(2)));
typedef float f32x4 __attribute__((ext_vector_type(4)));
typedef float f32x2 __attribute__((ext_vector_type(2)));
typedef short v4s __attribute__((ext_vector_type(4)));
typedef __bf16 bf16x8 __attribute__((ext_vector_type(8)));
typedef __bf16 bf16x2 __attribute__((ext_vector_type(2)));
#define LAS __attribute__((address_space(3)))
#define GAS __attribute__((address_space(1)))
typedef const unsigned char* cptr;

constexpr int S_ = 2048, D_ = 640, F_ = 1536;
constexpr size_t OF_OFF = 0;
constexpr size_t HF_OFF = OF_OFF + 2621440;
constexpr size_t HR_OFF = HF_OFF + 2621440;
constexpr size_t UF_OFF = HR_OFF + 2621440;
constexpr size_t ST_OFF = UF_OFF + 1966080;
constexpr float EPS = 1e-5f;

#define WAITV(n) asm volatile("s_waitcnt vmcnt(" #n ")" ::: "memory")
#define WAITL0() asm volatile("s_waitcnt lgkmcnt(0)" ::: "memory")
#define BAR() asm volatile("s_barrier" ::: "memory")

__device__ __forceinline__ void dma16(unsigned voff, cptr base, unsigned ldsaddr) {
  asm volatile("s_mov_b32 m0, %2\n\ts_nop 0\n\tglobal_load_lds_dwordx4 %0, %1" ::"v"(voff), "s"(base), "s"(ldsaddr) : "memory");
}
template <int OFF> __device__ __forceinline__ void gld16(v4u& dst, unsigned voff, cptr base) {
  asm volatile("global_load_dwordx4 %0, %1, %2 offset:%3" : "=v"(dst) : "v"(voff), "s"(base), "i"(OFF) : "memory");
}
#define PIN(x) asm volatile("" : "+v"(x))

__device__ __forceinline__ v4u lds128(unsigned a) { return *(const LAS v4u*)(size_t)a; }
__device__ __forceinline__ v2u lds64(unsigned a) { return *(const LAS v2u*)(size_t)a; }
__device__ __forceinline__ float lds32f(unsigned a) { return *(const LAS float*)(size_t)a; }
__device__ __forceinline__ void sts128(unsigned a, v4u v) { *(LAS v4u*)(size_t)a = v; }
__device__ __forceinline__ void sts64(unsigned a, v2u v) { *(LAS v2u*)(size_t)a = v; }
__device__ __forceinline__ void sts32f(unsigned a, float v) { *(LAS float*)(size_t)a = v; }
__device__ __forceinline__ v2u ldstr(unsigned a) {
  return __builtin_bit_cast(v2u, __builtin_amdgcn_ds_read_tr16_b64_v4i16((LAS v4s*)(size_t)a));
}
__device__ __forceinline__ v4u cat(v2u a, v2u b) { return v4u{a.x, a.y, b.x, b.y}; }
__device__ __forceinline__ f32x4 mfma(v4u a, v4u b, f32x4 c) {
  return __builtin_amdgcn_mfma_f32_16x16x32_bf16(__builtin_bit_cast(bf16x8, a), __builtin_bit_cast(bf16x8, b), c, 0, 0, 0);
}
__device__ __forceinline__ float dot2(unsigned a, unsigned b, float c) {
  return __builtin_amdgcn_fdot2_f32_bf16(__builtin_bit_cast(bf16x2, a), __builtin_bit_cast(bf16x2, b), c, false);
}
__device__ __forceinline__ unsigned pk(float lo, float hi) {
  return __builtin_bit_cast(unsigned, __builtin_convertvector(f32x2{lo, hi}, bf16x2));
}
__device__ __forceinline__ float blo(unsigned u) { return __builtin_bit_cast(float, u << 16); }
__device__ __forceinline__ float bhi(unsigned u) { return __builtin_bit_cast(float, u & 0xffff0000u); }
__device__ __forceinline__ float dppx1(float v) {
  return __builtin_bit_cast(float, __builtin_amdgcn_mov_dpp(__builtin_bit_cast(int, v), 0xB1, 0xF, 0xF, true));
}
__device__ __forceinline__ unsigned swap23(unsigned r) { return (r & ~12u) | ((r & 4u) << 1) | ((r & 8u) >> 1); }
constexpr unsigned ONES = 0x3f803f80u;

constexpr int K1_STAGE = 40960;
constexpr int QP = 576;
__global__ __launch_bounds__(512, 2) void k1_qkv_attn(cptr x, cptr wqkv, unsigned char* ws) {
  extern __shared__ __attribute__((aligned(16))) unsigned char smem[];
  const int tid = threadIdx.x, lane = tid & 63, w = __builtin_amdgcn_readfirstlane(tid >> 6);
  const int b = blockIdx.x, xcd = b & 7, idx = b >> 3;
  const int tt = (xcd >> 1) * 8 + (idx & 7), hg = (xcd & 1) * 4 + (idx >> 3);
  const int s0 = tt * 64;

  unsigned voff[5];
#pragma unroll
  for (int i = 0; i < 5; ++i) {
    const int id = w + 8 * i;
    if (i == 0 || (i == 1 && w < 2)) {
      const int row = 8 * id + (lane >> 3), ch = (lane & 7) ^ ((row >> 1) & 7);
      int tok = s0 - 16 + row;
      tok = tok < 0 ? 0 : tok;
      voff[i] = tok * 1280 + ch * 16;
    } else {
      const unsigned c = (id - 10) * 64 + lane, R = (c * 6554u) >> 16, ch = c - R * 10u;
      voff[i] = swap23(R & 63u) * 3840u + (R >> 6) * 1280u + hg * 160 + ch * 16;
    }
  }
  auto issue = [&](int s) {
    cptr bx = x + s * 128, bw = wqkv + (size_t)s * (64 * 3840);
    const unsigned l = (s & 3) * K1_STAGE + w * 1024;
    dma16(voff[0], bx, l);
    dma16(voff[1], w < 2 ? bx : bw, l + 8192);
    dma16(voff[2], bw, l + 16384);
    dma16(voff[3], bw, l + 24576);
    dma16(voff[4], bw, l + 32768);
  };
  issue(0); issue(1); issue(2);

  if (hg == 0 && tid < 64) *(GAS v4u*)(ws + ST_OFF + (size_t)(s0 + tid) * 16) = v4u{0u, 0u, 0u, 0u};

  const int g = lane >> 4, r16 = lane & 15, q4 = (lane & 15) >> 2, p4 = lane & 3;
  const int ct0 = w, ct1 = w < 7 ? w + 8 : 7;
  const unsigned wl = 10240 + ((g >> 1) * 16 + (g & 1) * 4 + q4) * 160 + p4 * 8;
  const unsigned wa0 = wl + (ct0 / 5) * 10240 + (ct0 % 5) * 32, wa1 = wl + (ct1 / 5) * 10240 + (ct1 % 5) * 32;
  const unsigned xa0 = r16 * 128 + ((g ^ ((r16 >> 1) & 7)) * 16), xa1 = xa0 ^ 64u;

  f32x4 acc[2][5];
#pragma unroll
  for (int i = 0; i < 2; ++i)
#pragma unroll
    for (int j = 0; j < 5; ++j) acc[i][j] = f32x4{0.f, 0.f, 0.f, 0.f};
  float cs0 = 0.f, cs1 = 0.f, sm0 = 0.f, sq0 = 0.f, sm1 = 0.f, sq1 = 0.f;

  for (int s = 0; s < 10; ++s) {
    if (s < 8) WAITV(10); else if (s == 8) WAITV(5); else WAITV(0);
    WAITL0();
    BAR();
    if (s < 7) issue(s + 3);
    const unsigned sb = (s & 3) * K1_STAGE;
#pragma unroll
    for (int kk = 0; kk < 2; ++kk) {
      const v4u a0 = cat(ldstr(sb + wa0 + kk * 5120), ldstr(sb + wa0 + kk * 5120 + 1280));
      const v4u a1 = cat(ldstr(sb + wa1 + kk * 5120), ldstr(sb + wa1 + kk * 5120 + 1280));
      v4u xb[5];
#pragma unroll
      for (int j = 0; j < 5; ++j) xb[j] = lds128(sb + (kk ? xa1 : xa0) + j * 2048);
#pragma unroll
      for (int j = 0; j < 5; ++j) {
        acc[0][j] = mfma(a0, xb[j], acc[0][j]);
        acc[1][j] = mfma(a1, xb[j], acc[1][j]);
      }
#pragma unroll
      for (int e = 0; e < 4; ++e) { cs0 = dot2(a0[e], ONES, cs0); cs1 = dot2(a1[e], ONES, cs1); }
    }
    {
      const v4u sx = lds128(sb + tid * 16);
#pragma unroll
      for (int e = 0; e < 4; ++e) { sm0 = dot2(sx[e], ONES, sm0); sq0 = dot2(sx[e], sx[e], sq0); }
      if (w < 2) {
        const v4u sy = lds128(sb + 8192 + tid * 16);
#pragma unroll
        for (int e = 0; e < 4; ++e) { sm1 = dot2(sy[e], ONES, sm1); sq1 = dot2(sy[e], sy[e], sq1); }
      }
    }
  }
  constexpr unsigned MEAN = 0, RSTD = 320, CSUM = 640, QKV = 81920, OIM = 131072;
#pragma unroll
  for (int m = 1; m < 8; m <<= 1) {
    sm0 += __shfl_xor(sm0, m); sq0 += __shfl_xor(sq0, m);
    sm1 += __shfl_xor(sm1, m); sq1 += __shfl_xor(sq1, m);
  }
  cs0 += __shfl_xor(cs0, 16); cs1 += __shfl_xor(cs1, 16);
  cs0 += __shfl_xor(cs0, 32); cs1 += __shfl_xor(cs1, 32);
  if ((lane & 7) == 0) {
    {
      const float mean = sm0 * (1.f / D_), var = fmaxf(sq0 * (1.f / D_) - mean * mean, 0.f);
      sts32f(MEAN + (tid >> 3) * 4, mean);
      sts32f(RSTD + (tid >> 3) * 4, 1.f / sqrtf(var + EPS));
    }
    if (w < 2) {
      const float mean = sm1 * (1.f / D_), var = fmaxf(sq1 * (1.f / D_) - mean * mean, 0.f);
      sts32f(MEAN + (64 + (tid >> 3)) * 4, mean);
      sts32f(RSTD + (64 + (tid >> 3)) * 4, 1.f / sqrtf(var + EPS));
    }
  }
  if (lane < 16) { sts32f(CSUM + (ct0 * 16 + lane) * 4, cs0); sts32f(CSUM + (ct1 * 16 + lane) * 4, cs1); }
  __syncthreads();
#pragma unroll
  for (int i = 0; i < 2; ++i) {
    const int ct = i ? ct1 : ct0;
    const f32x4 cs = __builtin_bit_cast(f32x4, lds128(CSUM + (ct * 16 + g * 4) * 4));
    const int ch = (ct % 5) * 16 + g * 4, hd = ch / 20, dm = ch - hd * 20;
    const unsigned dst = QKV + r16 * QP + (ct / 5) * 192 + hd * 48 + dm * 2;
#pragma unroll
    for (int j = 0; j < 5; ++j) {
      const float mean = lds32f(MEAN + (j * 16 + r16) * 4), rs = lds32f(RSTD + (j * 16 + r16) * 4);
      const f32x4 a = acc[i][j];
      sts64(dst + j * 16 * QP, v2u{pk((a.x - mean * cs.x) * rs, (a.y - mean * cs.y) * rs), pk((a.z - mean * cs.z) * rs, (a.w - mean * cs.w) * rs)});
    }
  }
  __syncthreads();
  {
    const int tl = w * 8 + (lane >> 3), hd = (lane >> 1) & 3, hf = lane & 1;
    const unsigned qa = QKV + (16 + tl) * QP + hd * 48;
    unsigned qv[10];
    {
      const v4u a = lds128(qa), bq = lds128(qa + 16); const v2u c = lds64(qa + 32);
      qv[0] = a.x; qv[1] = a.y; qv[2] = a.z; qv[3] = a.w; qv[4] = bq.x; qv[5] = bq.y; qv[6] = bq.z; qv[7] = bq.w; qv[8] = c.x; qv[9] = c.y;
    }
    const unsigned ka = qa + 192 - hf * 2 * QP;
    float lg[4];
#pragma unroll
    for (int i = 0; i < 4; ++i) {
      const unsigned a_ = ka - i * 4 * QP;
      const v4u a = lds128(a_), bq = lds128(a_ + 16); const v2u c = lds64(a_ + 32);
      float d = dot2(a.x, qv[0], 0.f);
      d = dot2(a.y, qv[1], d); d = dot2(a.z, qv[2], d); d = dot2(a.w, qv[3], d);
      d = dot2(bq.x, qv[4], d); d = dot2(bq.y, qv[5], d); d = dot2(bq.z, qv[6], d); d = dot2(bq.w, qv[7], d);
      d = dot2(c.x, qv[8], d); d = dot2(c.y, qv[9], d);
      d *= 0.22360679774997896f;
      lg[i] = (s0 + tl - 4 * i - 2 * hf >= 0) ? d : -1e9f;
    }
    float mx = fmaxf(fmaxf(lg[0], lg[1]), fmaxf(lg[2], lg[3]));
    mx = fmaxf(mx, dppx1(mx));
    float p[4], sum = 0.f;
#pragma unroll
    for (int i = 0; i < 4; ++i) { p[i] = __expf(lg[i] - mx); sum += p[i]; }
    sum += dppx1(sum);
    const float inv = 1.f / sum;
    float o[20];
#pragma unroll
    for (int d = 0; d < 20; ++d) o[d] = 0.f;
#pragma unroll
    for (int i = 0; i < 4; ++i) {
      const unsigned a_ = ka + 192 - i * 4 * QP;
      const v4u a = lds128(a_), bq = lds128(a_ + 16); const v2u c = lds64(a_ + 32);
      const unsigned vv[10] = {a.x, a.y, a.z, a.w, bq.x, bq.y, bq.z, bq.w, c.x, c.y};
      const float pi = p[i] * inv;
#pragma unroll
      for (int d = 0; d < 10; ++d) { o[2 * d] = fmaf(pi, blo(vv[d]), o[2 * d]); o[2 * d + 1] = fmaf(pi, bhi(vv[d]), o[2 * d + 1]); }
    }
#pragma unroll
    for (int d = 0; d < 20; ++d) o[d] += dppx1(o[d]);
    const unsigned oa = OIM + tl * 176 + hd * 40;
    if (hf == 0) {
#pragma unroll
      for (int d = 0; d < 5; ++d) sts64(oa + d * 8, v2u{pk(o[4 * d], o[4 * d + 1]), pk(o[4 * d + 2], o[4 * d + 3])});
    }
    WAITL0();
    const int tk = w * 8 + (lane & 7), tg = s0 + tk;
#pragma unroll
    for (int pass = 0; pass < 2; ++pass) {
      const int ci = pass * 8 + (lane >> 3);
      if (pass == 0 || lane < 16) {
        const v4u v = lds128(OIM + tk * 176 + ci * 16);
        const int c8 = hg * 10 + ci;
        *(GAS v4u*)(ws + OF_OFF + (size_t)((tg >> 4) * 20 + (c8 >> 2)) * 1024 + ((c8 & 3) * 16 + (tg & 15)) * 16) = v;
      }
    }
  }
}

__global__ __launch_bounds__(512, 2) void k2_oproj(cptr x, cptr wo, unsigned char* ws) {
  extern __shared__ __attribute__((aligned(16))) unsigned char smem[];
  const int tid = threadIdx.x, lane = tid & 63, w = __builtin_amdgcn_readfirstlane(tid >> 6);
  const int b = blockIdx.x, xcd = b & 7, idx = b >> 3;
  const int st = (xcd >> 1) * 8 + (idx & 7), dt = (xcd & 1) * 4 + (idx >> 3);
  const int kh = w >> 2, tt = w & 3;
  const int g = lane >> 4, r16 = lane & 15, q4 = (lane & 15) >> 2, p4 = lane & 3;

  const int etok = ((lane >> 5) * 16 + r16), ehalf = (lane >> 4) & 1;
  v4u xr[2];
  {
    const unsigned xo = (st * 64 + (w & 1) * 32 + etok) * 1280 + dt * 160 + ehalf * 16;
    gld16<0>(xr[0], xo + (w >> 1) * 32, x);
    gld16<128>(xr[1], xo, x);
  }
  cptr ob = ws + OF_OFF + (size_t)((st * 4 + tt) * 20 + kh * 10) * 1024;
  const unsigned lo = lane * 16;
  v4u of[10];
  auto round = [&](int i) {
    const unsigned p = 8 * i + w, c = p * 64 + lane, R = (c * 6554u) >> 16, ch = c - R * 10u, q = R >> 5;
    const unsigned k = ((q & 1) * 10 + (q >> 1)) * 32 + swap23(R & 31u);
    dma16(k * 1280u + dt * 160 + ch * 16, wo, p * 1024);
  };
  gld16<0>(of[0], lo, ob); gld16<1024>(of[1], lo, ob); gld16<2048>(of[2], lo, ob); gld16<3072>(of[3], lo, ob);
  round(0); round(1); round(2); round(3); round(4);
  gld16<0>(of[4], lo, ob + 4096); gld16<1024>(of[5], lo, ob + 4096); gld16<2048>(of[6], lo, ob + 4096); gld16<3072>(of[7], lo, ob + 4096);
  round(5); round(6); round(7); round(8); round(9);
  gld16<0>(of[8], lo, ob + 8192); gld16<1024>(of[9], lo, ob + 8192);
  round(10); round(11);
  if (w < 4) round(12);

  const unsigned wl = kh * 5120 + ((g >> 1) * 16 + (g & 1) * 4 + q4) * 160 + p4 * 8;
  f32x4 acc[5];
#pragma unroll
  for (int c = 0; c < 5; ++c) acc[c] = f32x4{0.f, 0.f, 0.f, 0.f};
  auto kstep = [&](int j) {
#pragma unroll
    for (int c = 0; c < 5; ++c) {
      const unsigned a = wl + j * 10240 + c * 32;
      acc[c] = mfma(cat(ldstr(a), ldstr(a + 1280)), of[j], acc[c]);
    }
  };
  WAITV(13); BAR();
  PIN(of[0]); PIN(of[1]); PIN(of[2]); PIN(of[3]);
  kstep(0); kstep(1); kstep(2); kstep(3);
  WAITV(4); BAR();
  PIN(of[4]); PIN(of[5]); PIN(of[6]); PIN(of[7]);
  kstep(4); kstep(5); kstep(6); kstep(7);
  WAITV(0); BAR();
  PIN(of[8]); PIN(of[9]); PIN(xr[0]); PIN(xr[1]);
  kstep(8); kstep(9);

  constexpr unsigned RED = 102400, HIM = RED + 40960, PST = HIM + 64 * 160;
#pragma unroll
  for (int c = 0; c < 5; ++c) sts128(RED + ((kh * 5 + c) * 4 + tt) * 1024 + lo, __builtin_bit_cast(v4u, acc[c]));
  __syncthreads();
#pragma unroll
  for (int pass = 0; pass < 2; ++pass) {
    if (pass == 0 || w < 2) {
      const int P = pass * 8 + w, ct = P >> 1, ttile = (P & 1) * 2 + (lane >> 5);
      const unsigned ra = RED + (ct * 4 + ttile) * 1024 + (ehalf * 32 + r16) * 16;
      const f32x4 a0 = __builtin_bit_cast(f32x4, lds128(ra)) + __builtin_bit_cast(f32x4, lds128(ra + 20480));
      const f32x4 a1 = __builtin_bit_cast(f32x4, lds128(ra + 256)) + __builtin_bit_cast(f32x4, lds128(ra + 256 + 20480));
      const v4u xv = xr[pass];
      v4u h;
      h.x = pk(a0.x + blo(xv.x), a0.y + bhi(xv.x)); h.y = pk(a0.z + blo(xv.y), a0.w + bhi(xv.y));
      h.z = pk(a1.x + blo(xv.z), a1.y + bhi(xv.z)); h.w = pk(a1.z + blo(xv.w), a1.w + bhi(xv.w));
      *(GAS v4u*)(ws + HR_OFF + (size_t)((st * 8 + dt) * 10 + P) * 1024 + lo) = h;
      const int tok = (P & 1) * 32 + etok;
      sts128(HIM + swap23(tok) * 160 + ct * 32 + ehalf * 16, h);
      float sm = 0.f, sq = 0.f;
#pragma unroll
      for (int e = 0; e < 4; ++e) { sm = dot2(h[e], ONES, sm); sq = dot2(h[e], h[e], sq); }
      sts64(PST + (tok * 10 + ct * 2 + ehalf) * 8, v2u{__builtin_bit_cast(unsigned, sm), __builtin_bit_cast(unsigned, sq)});
    }
  }
  __syncthreads();
#pragma unroll
  for (int pass = 0; pass < 2; ++pass) {
    if (pass == 0 || w < 2) {
      const int P = pass * 8 + w, ct = P >> 1, ks = P & 1;
      const unsigned a = HIM + ks * 5120 + ((g >> 1) * 16 + (g & 1) * 4 + q4) * 160 + p4 * 8 + ct * 32;
      const v4u v = cat(ldstr(a), ldstr(a + 1280));
      *(GAS v4u*)(ws + HF_OFF + (size_t)((dt * 5 + ct) * 64 + st * 2 + ks) * 1024 + lo) = v;
    }
  }
  if (w == 7 || w == 6) {
    float s = 0.f;
#pragma unroll
    for (int i = 0; i < 10; ++i) s += lds32f(PST + (lane * 10 + i) * 8 + (w & 1) * 4);
    __hip_atomic_fetch_add((GAS double*)(ws + ST_OFF) + (st * 64 + lane) * 2 + (w & 1), (double)s, __ATOMIC_RELAXED, __HIP_MEMORY_SCOPE_AGENT);
  }
}

__device__ __forceinline__ float gelu_tanh(float v) {
  const float z = v * (1.5957691216057308f + 0.07135481282948586f * v * v);
  return v / (1.f + __expf(-z));
}
__global__ __launch_bounds__(512, 2) void k3_mix1(cptr w1, cptr b1, unsigned char* ws) {
  extern __shared__ __attribute__((aligned(16))) unsigned char smem[];
  const int tid = threadIdx.x, lane = tid & 63, w = __builtin_amdgcn_readfirstlane(tid >> 6);
  const int b = blockIdx.x, xcd = b & 7, idx = b >> 3;
  const int dt = (xcd & 1) * 5 + idx % 5, ft = (xcd >> 1) * 6 + idx / 5;
  const int g = lane >> 4, r16 = lane & 15, q4 = (lane & 15) >> 2, p4 = lane & 3;
  const unsigned lo = lane * 16;

  v4u sv[4], bias;
  {
    cptr sp = ws + ST_OFF + (size_t)w * 4096;
    gld16<0>(sv[0], lane * 64, sp); gld16<16>(sv[1], lane * 64, sp); gld16<32>(sv[2], lane * 64, sp); gld16<48>(sv[3], lane * 64, sp);
    gld16<0>(bias, (ft * 64 + (w >> 2) * 32 + g * 8) * 2, b1);
  }
  unsigned wv[4];
#pragma unroll
  for (int p = 0; p < 4; ++p) {
    const unsigned row = 8 * p + (lane >> 3), f = ((row >> 1) & 1) | ((p & 1) << 1);
    wv[p] = row * 3072u + (((lane & 7) ^ (f << 1)) * 16);
  }
  cptr wb = w1 + (size_t)w * (256 * 3072) + ft * 128;
  cptr hb = ws + HF_OFF + (size_t)(dt * 4 * 64 + w * 8) * 1024;
  const unsigned ring = w * 16384;
  v4u hf[4][4];
  auto issue = [&](int kk) {
#pragma unroll
    for (int p = 0; p < 4; ++p) dma16(wv[p], wb + kk * (32 * 3072), ring + (kk & 3) * 4096 + p * 1024);
#pragma unroll
    for (int j = 0; j < 4; ++j) gld16<0>(hf[kk & 3][j], lo, hb + (size_t)j * 65536 + kk * 1024);
  };
  issue(0); issue(1); issue(2); issue(3);

  const unsigned SA = 131072 + w * 2048, SB = SA + 1024;
  WAITV(33);
  {
    f32x4 a, c;
#pragma unroll
    for (int i = 0; i < 4; ++i) {
      PIN(sv[i]);
      const double sm = __builtin_bit_cast(double, v2u{sv[i].x, sv[i].y}), sq = __builtin_bit_cast(double, v2u{sv[i].z, sv[i].w});
      const double md = sm * (1.0 / D_);
      const float mean = (float)md, var = fmaxf((float)(sq * (1.0 / D_) - md * md), 0.f);
      const float r1 = 1.f / sqrtf(var + EPS), r2 = 1.f / sqrtf(var * r1 * r1 + EPS);
      a[i] = r1 * r2; c[i] = -mean * r1 * r2;
    }
    sts128(SA + lo, __builtin_bit_cast(v4u, a));
    sts128(SB + lo, __builtin_bit_cast(v4u, c));
  }
  const unsigned fl = ((q4 >> 1) & 1) | ((g & 1) << 1);
  const unsigned ta = ring + (8 * g + q4) * 128 + fl * 32 + p4 * 8;
  f32x4 acc[4][4];
#pragma unroll
  for (int i = 0; i < 4; ++i)
#pragma unroll
    for (int j = 0; j < 4; ++j) acc[i][j] = f32x4{0.f, 0.f, 0.f, 0.f};
#pragma unroll
  for (int kk = 0; kk < 8; ++kk) {
    if (kk <= 4) WAITV(24); else if (kk == 5) WAITV(16); else if (kk == 6) WAITV(8); else WAITV(0);
    v4u wf[4], hn[4];
#pragma unroll
    for (int i = 0; i < 4; ++i) {
      const unsigned a = (ta ^ (i * 32)) + (kk & 3) * 4096;
      wf[i] = cat(ldstr(a), ldstr(a + 512));
    }
    const f32x4 a0 = __builtin_bit_cast(f32x4, lds128(SA + kk * 128 + g * 32)), a1 = __builtin_bit_cast(f32x4, lds128(SA + kk * 128 + g * 32 + 16));
    const f32x4 c0 = __builtin_bit_cast(f32x4, lds128(SB + kk * 128 + g * 32)), c1 = __builtin_bit_cast(f32x4, lds128(SB + kk * 128 + g * 32 + 16));
#pragma unroll
    for (int j = 0; j < 4; ++j) {
      PIN(hf[kk & 3][j]);
      const v4u h = hf[kk & 3][j];
      hn[j].x = pk(fmaf(blo(h.x), a0.x, c0.x), fmaf(bhi(h.x), a0.y, c0.y));
      hn[j].y = pk(fmaf(blo(h.y), a0.z, c0.z), fmaf(bhi(h.y), a0.w, c0.w));
      hn[j].z = pk(fmaf(blo(h.z), a1.x, c1.x), fmaf(bhi(h.z), a1.y, c1.y));
      hn[j].w = pk(fmaf(blo(h.w), a1.z, c1.z), fmaf(bhi(h.w), a1.w, c1.w));
    }
    if (kk < 4) {
#pragma unroll
      for (int j = 0; j < 4; ++j) PIN(hn[j]);
      WAITL0();
      issue(kk + 4);
    }
#pragma unroll
    for (int i = 0; i < 4; ++i)
#pragma unroll
      for (int j = 0; j < 4; ++j) acc[i][j] = mfma(wf[i], hn[j], acc[i][j]);
  }
#pragma unroll
  for (int i = 0; i < 4; ++i)
#pragma unroll
    for (int j = 0; j < 4; ++j) sts128(ring + (i * 4 + j) * 1024 + lo, __builtin_bit_cast(v4u, acc[i][j]));
  __syncthreads();
  {
    const int dj = w & 3, fk = w >> 2;
    const unsigned ra = ((2 * fk + (g >> 1)) * 4 + dj) * 1024 + ((g & 1) * 32 + r16) * 16;
    f32x4 s0 = f32x4{0.f, 0.f, 0.f, 0.f}, s1 = s0;
#pragma unroll
    for (int v = 0; v < 8; ++v) {
      s0 += __builtin_bit_cast(f32x4, lds128(ra + v * 16384));
      s1 += __builtin_bit_cast(f32x4, lds128(ra + v * 16384 + 256));
    }
    PIN(bias);
    v4u u;
    u.x = pk(gelu_tanh(s0.x + blo(bias.x)), gelu_tanh(s0.y + bhi(bias.x)));
    u.y = pk(gelu_tanh(s0.z + blo(bias.y)), gelu_tanh(s0.w + bhi(bias.y)));
    u.z = pk(gelu_tanh(s1.x + blo(bias.z)), gelu_tanh(s1.y + bhi(bias.z)));
    u.w = pk(gelu_tanh(s1.z + blo(bias.w)), gelu_tanh(s1.w + bhi(bias.w)));
    *(GAS v4u*)(ws + UF_OFF + (size_t)((dt * 4 + dj) * 48 + ft * 2 + fk) * 1024 + lo) = u;
  }
}

__global__ __launch_bounds__(512, 2) void k4_mix2(cptr w2, cptr b2, unsigned char* ws, unsigned char* out) {
  extern __shared__ __attribute__((aligned(16))) unsigned char smem[];
  const int tid = threadIdx.x, lane = tid & 63, w = __builtin_amdgcn_readfirstlane(tid >> 6);
  const int b = blockIdx.x, xcd = b & 7, idx = b >> 3;
  const int st = (xcd >> 1) * 8 + (idx & 7), dt = (xcd & 1) * 4 + (idx >> 3);
  const int g = lane >> 4, r16 = lane & 15, q4 = (lane & 15) >> 2, p4 = lane & 3;
  const unsigned lo = lane * 16;
  const int etok = ((lane >> 5) * 16 + r16), ehalf = (lane >> 4) & 1;

  v4u hr[2];
  unsigned bq;
  {
    cptr hp = ws + HR_OFF + (size_t)((st * 8 + dt) * 10) * 1024;
    gld16<0>(hr[0], lo + w * 1024, hp);
    gld16<0>(hr[1], lo + (8 + (w & 1)) * 1024, hp);
    asm volatile("global_load_ushort %0, %1, %2" : "=v"(bq) : "v"((st * 64 + (w & 1) * 32 + etok) * 2), "s"(b2) : "memory");
  }
  unsigned wv[4];
#pragma unroll
  for (int p = 0; p < 4; ++p) {
    const unsigned row = 8 * p + (lane >> 3), f = ((row >> 1) & 1) | ((p & 1) << 1);
    wv[p] = row * 4096u + (((lane & 7) ^ (f << 1)) * 16);
  }
  cptr wb = w2 + (size_t)w * (192 * 4096) + st * 128;
  cptr ub = ws + UF_OFF + (size_t)(dt * 5 * 48 + w * 6) * 1024;
  const unsigned ring = w * 20480;
  v4u uf[4][5];
  auto issue = [&](int kk) {
#pragma unroll
    for (int p = 0; p < 4; ++p) dma16(wv[p], wb + kk * (32 * 4096), ring + (kk & 3) * 4096 + p * 1024);
#pragma unroll
    for (int j = 0; j < 5; ++j) gld16<0>(uf[kk & 3][j], lo, ub + (size_t)j * 49152 + kk * 1024);
  };
  issue(0); issue(1); issue(2); issue(3);
  const unsigned fl = ((q4 >> 1) & 1) | ((g & 1) << 1);
  const unsigned ta = ring + (8 * g + q4) * 128 + fl * 32 + p4 * 8;
  f32x4 acc[5][4];
#pragma unroll
  for (int i = 0; i < 5; ++i)
#pragma unroll
    for (int j = 0; j < 4; ++j) acc[i][j] = f32x4{0.f, 0.f, 0.f, 0.f};
#pragma unroll
  for (int kk = 0; kk < 6; ++kk) {
    if (kk <= 2) WAITV(27); else if (kk == 3) WAITV(18); else if (kk == 4) WAITV(9); else WAITV(0);
    v4u wf[4];
#pragma unroll
    for (int j = 0; j < 4; ++j) {
      const unsigned a = (ta ^ (j * 32)) + (kk & 3) * 4096;
      wf[j] = cat(ldstr(a), ldstr(a + 512));
    }
#pragma unroll
    for (int i = 0; i < 5; ++i) PIN(uf[kk & 3][i]);
#pragma unroll
    for (int i = 0; i < 5; ++i)
#pragma unroll
      for (int j = 0; j < 4; ++j) acc[i][j] = mfma(uf[kk & 3][i], wf[j], acc[i][j]);
    if (kk < 2) {
#pragma unroll
      for (int i = 0; i < 5; ++i) PIN(acc[i][3]);
      WAITL0();
      issue(kk + 4);
    }
  }
#pragma unroll
  for (int i = 0; i < 5; ++i)
#pragma unroll
    for (int j = 0; j < 4; ++j) sts128(ring + (i * 4 + j) * 1024 + lo, __builtin_bit_cast(v4u, acc[i][j]));
  __syncthreads();
  PIN(hr[0]); PIN(hr[1]); PIN(bq);
  const float bias = blo(bq);
#pragma unroll
  for (int pass = 0; pass < 2; ++pass) {
    if (pass == 0 || w < 2) {
      const int P = pass * 8 + w, ct = P >> 1, ttile = (P & 1) * 2 + (lane >> 5);
      const unsigned ra = (ct * 4 + ttile) * 1024 + (ehalf * 32 + r16) * 16;
      f32x4 s0 = f32x4{0.f, 0.f, 0.f, 0.f}, s1 = s0;
#pragma unroll
      for (int v = 0; v < 8; ++v) {
        s0 += __builtin_bit_cast(f32x4, lds128(ra + v * 20480));
        s1 += __builtin_bit_cast(f32x4, lds128(ra + v * 20480 + 256));
      }
      const v4u hv = hr[pass];
      v4u o;
      o.x = pk(s0.x + bias + blo(hv.x), s0.y + bias + bhi(hv.x)); o.y = pk(s0.z + bias + blo(hv.y), s0.w + bias + bhi(hv.y));
      o.z = pk(s1.x + bias + blo(hv.z), s1.y + bias + bhi(hv.z)); o.w = pk(s1.z + bias + blo(hv.w), s1.w + bias + bhi(hv.w));
      *(GAS v4u*)(out + (size_t)(st * 64 + (P & 1) * 32 + etok) * 1280 + dt * 160 + ct * 32 + ehalf * 16) = o;
    }
  }
}

extern "C" void kernel_launch(void* const* d_in, const int* in_sizes, int n_in, void* d_out, int out_size, void* d_ws,
                              size_t ws_size, hipStream_t stream) {
  cptr x = (cptr)d_in[0], wqkv = (cptr)d_in[1], wo = (cptr)d_in[2];
  cptr w1 = (cptr)d_in[4], b1 = (cptr)d_in[5], w2 = (cptr)d_in[6], b2 = (cptr)d_in[7];
  unsigned char* ws = (unsigned char*)d_ws;
  (void)hipFuncSetAttribute((const void*)k1_qkv_attn, hipFuncAttributeMaxDynamicSharedMemorySize, 163840);
  (void)hipFuncSetAttribute((const void*)k2_oproj, hipFuncAttributeMaxDynamicSharedMemorySize, 163840);
  (void)hipFuncSetAttribute((const void*)k3_mix1, hipFuncAttributeMaxDynamicSharedMemorySize, 163840);
  (void)hipFuncSetAttribute((const void*)k4_mix2, hipFuncAttributeMaxDynamicSharedMemorySize, 163840);
  k1_qkv_attn<<<256, 512, 163840, stream>>>(x, wqkv, ws);
  k2_oproj<<<256, 512, 163840, stream>>>(x, wo, ws);
  k3_mix1<<<240, 512, 147456, stream>>>(w1, b1, ws);
  k4_mix2<<<256, 512, 163840, stream>>>(w2, b2, ws, (unsigned char*)d_out);
}
```

```cpp
#include <hip/hip_runtime.h>
#include <hip/hip_bf16.h>

typedef unsigned v4u __attribute__((ext_vector_type(4)));
typedef unsigned v2u __attribute__((ext_vector_type(2)));
typedef float f32x4 __attribute__((ext_vector_type(4)));
typedef float f32x2 __attribute__((ext_vector_type(2)));
typedef short v4s __attribute__((ext_vector_type(4)));
typedef __bf16 bf16x8 __attribute__((ext_vector_type(8)));
typedef __bf16 bf16x2 __attribute__((ext_vector_type(2)));
#define LAS __attribute__((address_space(3)))
#define GAS __attribute__((address_space(1)))
typedef const unsigned char* cptr;

constexpr int D_ = 640;
constexpr unsigned OF_OFF = 0;
constexpr unsigned HF_OFF = OF_OFF + 2621440;
constexpr unsigned UF_OFF = HF_OFF + 2 * 2621440;
constexpr unsigned ST_OFF = UF_OFF + 1966080;
constexpr unsigned F1_OFF = ST_OFF + 32768;
constexpr unsigned F2_OFF = F1_OFF + 4096;
constexpr unsigned F3_OFF = F2_OFF + 1024;
constexpr unsigned EP_OFF = F3_OFF + 2048;
constexpr unsigned WS_BYTES = EP_OFF + 256;
constexpr float EPS = 1e-5f;
constexpr unsigned SPIN_CAP = 1u << 16;

#define STAMP(k)

#define WAITV(n) asm volatile("s_waitcnt vmcnt(%0)" ::"i"(n) : "memory")
#define WAITL0() asm volatile("s_waitcnt lgkmcnt(0)" ::: "memory")
#define BAR() asm volatile("s_waitcnt lgkmcnt(0)\n\ts_barrier" ::: "memory")
#define PIN(x) asm volatile("" : "+v"(x))
#define SCHED() __builtin_amdgcn_sched_barrier(0)

__device__ __forceinline__ void dma16(unsigned voff, cptr base, unsigned ldsaddr) {
  asm volatile("s_mov_b32 m0, %2\n\ts_nop 0\n\tglobal_load_lds_dwordx4 %0, %1" ::"v"(voff), "s"(base), "s"(ldsaddr) : "memory");
}
__device__ __forceinline__ void gld16(v4u& dst, unsigned voff, cptr base) {
  asm volatile("global_load_dwordx4 %0, %1, %2" : "=v"(dst) : "v"(voff), "s"(base) : "memory");
}
template <int OFF> __device__ __forceinline__ void gld16c(v4u& dst, unsigned voff, cptr base) {
  asm volatile("global_load_dwordx4 %0, %1, %2 offset:%3 sc1" : "=v"(dst) : "v"(voff), "s"(base), "i"(OFF) : "memory");
}
__device__ __forceinline__ void gld4(unsigned& dst, unsigned voff, cptr base) {
  asm volatile("global_load_dword %0, %1, %2" : "=v"(dst) : "v"(voff), "s"(base) : "memory");
}

__device__ __forceinline__ v4u lds128(unsigned a) { return *(const LAS v4u*)(size_t)a; }
__device__ __forceinline__ f32x4 lds128f(unsigned a) { return *(const LAS f32x4*)(size_t)a; }
__device__ __forceinline__ v2u lds64(unsigned a) { return *(const LAS v2u*)(size_t)a; }
__device__ __forceinline__ f32x2 lds64f(unsigned a) { return *(const LAS f32x2*)(size_t)a; }
__device__ __forceinline__ float lds32f(unsigned a) { return *(const LAS float*)(size_t)a; }
__device__ __forceinline__ void sts128(unsigned a, v4u v) { *(LAS v4u*)(size_t)a = v; }
__device__ __forceinline__ void sts128f(unsigned a, f32x4 v) { *(LAS f32x4*)(size_t)a = v; }
__device__ __forceinline__ void sts64(unsigned a, v2u v) { *(LAS v2u*)(size_t)a = v; }
__device__ __forceinline__ void sts64f(unsigned a, f32x2 v) { *(LAS f32x2*)(size_t)a = v; }
__device__ __forceinline__ v2u ldstr(unsigned a) {
  return __builtin_bit_cast(v2u, __builtin_amdgcn_ds_read_tr16_b64_v4i16((LAS v4s*)(size_t)a));
}
__device__ __forceinline__ v4u cat(v2u a, v2u b) { return v4u{a.x, a.y, b.x, b.y}; }
__device__ __forceinline__ f32x4 mfma(v4u a, v4u b, f32x4 c) {
  return __builtin_amdgcn_mfma_f32_16x16x32_bf16(__builtin_bit_cast(bf16x8, a), __builtin_bit_cast(bf16x8, b), c, 0, 0, 0);
}
__device__ __forceinline__ float dot2(unsigned a, unsigned b, float c) {
  return __builtin_amdgcn_fdot2_f32_bf16(__builtin_bit_cast(bf16x2, a), __builtin_bit_cast(bf16x2, b), c, false);
}
__device__ __forceinline__ unsigned pk(float lo, float hi) {
  return __builtin_bit_cast(unsigned, __builtin_convertvector(f32x2{lo, hi}, bf16x2));
}
__device__ __forceinline__ float blo(unsigned u) { return __builtin_bit_cast(float, u << 16); }
__device__ __forceinline__ float bhi(unsigned u) { return __builtin_bit_cast(float, u & 0xffff0000u); }
template <int CTRL> __device__ __forceinline__ float dpp(float v) {
  return __builtin_bit_cast(float, __builtin_amdgcn_mov_dpp(__builtin_bit_cast(int, v), CTRL, 0xF, 0xF, true));
}
__device__ __forceinline__ float red8(float v) {
  v += dpp<0xB1>(v); v += dpp<0x4E>(v); v += dpp<0x141>(v);
  return v;
}
__device__ __forceinline__ float asf(unsigned u) { return __builtin_bit_cast(float, u); }
__device__ __forceinline__ unsigned asu(float f) { return __builtin_bit_cast(unsigned, f); }
__device__ __forceinline__ float rows_max(float v) {
  const v2u a = __builtin_amdgcn_permlane16_swap(asu(v), asu(v), false, false);
  v = fmaxf(asf(a.x), asf(a.y));
  const v2u c = __builtin_amdgcn_permlane32_swap(asu(v), asu(v), false, false);
  return fmaxf(asf(c.x), asf(c.y));
}
__device__ __forceinline__ float rows_sum(float v) {
  const v2u a = __builtin_amdgcn_permlane16_swap(asu(v), asu(v), false, false);
  v = asf(a.x) + asf(a.y);
  const v2u c = __builtin_amdgcn_permlane32_swap(asu(v), asu(v), false, false);
  return asf(c.x) + asf(c.y);
}
__device__ __forceinline__ unsigned swap23(unsigned r) { return (r & ~12u) | ((r & 4u) << 1) | ((r & 8u) >> 1); }
constexpr unsigned ONES = 0x3f803f80u;

__device__ __forceinline__ void xpose(v4u (&r)[4]) {
  unsigned lo[4][2], hi[4][2];
#pragma unroll
  for (int j = 0; j < 4; ++j)
#pragma unroll
    for (int q = 0; q < 2; ++q) {
      lo[j][q] = __builtin_amdgcn_update_dpp(r[j][q], r[j][2 + q], 0x128, 0xF, 0xC, false);
      hi[j][q] = __builtin_amdgcn_update_dpp(r[j][2 + q], r[j][q], 0x128, 0xF, 0x3, false);
    }
#pragma unroll
  for (int i = 0; i < 4; ++i) {
    const unsigned sel = (i & 1) ? 0x07060302u : 0x05040100u;
    r[i] = v4u{__builtin_amdgcn_perm(lo[1][i >> 1], lo[0][i >> 1], sel), __builtin_amdgcn_perm(lo[3][i >> 1], lo[2][i >> 1], sel),
               __builtin_amdgcn_perm(hi[1][i >> 1], hi[0][i >> 1], sel), __builtin_amdgcn_perm(hi[3][i >> 1], hi[2][i >> 1], sel)};
  }
}

__device__ __forceinline__ void wait_flags(cptr ws, unsigned voff, unsigned tag) {
  for (unsigned spin = 0; spin < SPIN_CAP; ++spin) {
    unsigned v;
    asm volatile("global_load_dword %0, %1, %2 sc1\n\ts_waitcnt vmcnt(0)" : "=v"(v) : "v"(voff), "s"(ws) : "memory");
    if (__builtin_amdgcn_ballot_w64(v != tag) == 0ull) break;
    __builtin_amdgcn_s_sleep(2);
  }
}
__device__ __forceinline__ float gelu_tanh(float v) {
  const float z = v * (-2.3022081986f - 0.10294324f * v * v);
  return v * __builtin_amdgcn_rcpf(1.f + __builtin_amdgcn_exp2f(z));
}

constexpr int STAGE = 40960;
constexpr int QP = 488, OP = 176;
constexpr int N1A = 12, N1B = 10;
constexpr int N2A = 8;

__global__ __launch_bounds__(512, 2) void fused_block(cptr x, cptr wqkv, cptr wo, cptr w1, cptr b1, cptr w2, cptr b2,
                                                      unsigned char* out, unsigned char* wsp) {
  extern __shared__ __attribute__((aligned(16))) unsigned char smem[];
  cptr ws = wsp;
  const __amdgpu_buffer_rsrc_t rs = __builtin_amdgcn_make_buffer_rsrc((void*)wsp, 0, (int)WS_BYTES, 0x00020000);
  const int tid = threadIdx.x, w = __builtin_amdgcn_readfirstlane(tid >> 6);
  int lane = tid & 63;
  const int b = blockIdx.x, xcd = b & 7, idx = b >> 3;
  const int st = (xcd >> 1) * 8 + (idx & 7), dt = (xcd & 1) * 4 + (idx >> 3);
  const int s0 = st * 64;
  int g = lane >> 4, r16 = lane & 15, q4 = (lane & 15) >> 2, p4 = lane & 3;
  unsigned lo = lane * 16;
  STAMP(0);

  unsigned ep;
  asm volatile("global_load_dword %0, %1, %2 sc1" : "=v"(ep) : "v"(EP_OFF), "s"(ws) : "memory");

  const int rot1 = ((idx & 7) + 3 * (idx >> 3)) % 10;
  unsigned voff[5];
#pragma unroll
  for (int i = 0; i < 5; ++i) {
    const int id = w + 8 * i;
    if (i == 0 || (i == 1 && w < 2)) {
      const int row = 8 * id + (lane >> 3), ch = (lane & 7) ^ ((row >> 1) & 7);
      int tok = s0 - 16 + row;
      tok = tok < 0 ? 0 : tok;
      voff[i] = tok * 1280 + ch * 16;
    } else {
      const unsigned c = (id - 10) * 64 + lane, R = (c * 6554u) >> 16, ch = c - R * 10u;
      voff[i] = swap23(R & 63u) * 3840u + (R >> 6) * 1280u + dt * 160 + ch * 16;
    }
  }
  auto issue = [&](int s) {
    int sr = s + rot1;
    sr = sr >= 10 ? sr - 10 : sr;
    cptr bx = x + sr * 128, bw = wqkv + (size_t)sr * (64 * 3840);
    const unsigned l = (s & 3) * STAGE + w * 1024;
    dma16(voff[0], bx, l);
    dma16(voff[1], w < 2 ? bx : bw, l + 8192);
    dma16(voff[2], bw, l + 16384);
    dma16(voff[3], bw, l + 24576);
    dma16(voff[4], bw, l + 32768);
  };
  const int rot2 = idx & 7;
  auto issue_wo = [&](int grp) {
#pragma unroll
    for (int i = 0; i < 5; ++i) {
      const unsigned p = grp * 40 + 8 * i + w;
      if (p < 100) {
        const unsigned c = p * 64 + lane, R = (c * 6554u) >> 16, ch = c - R * 10u, q = R >> 5;
        unsigned kq = (q >> 1) + rot2;
        kq = kq >= 10 ? kq - 10 : kq;
        const unsigned k = ((q & 1) * 10 + kq) * 32 + swap23(R & 31u);
        dma16(k * 1280u + dt * 160 + ch * 16, wo, ((grp + 2) & 3) * STAGE + (8 * i + w) * 1024);
      }
    }
  };
  issue(0);
  asm volatile("s_barrier" ::: "memory");
  issue(1);
  asm volatile("s_barrier" ::: "memory");
  issue(2);
  STAMP(1);
  if (dt == 0 && tid < 64) __builtin_amdgcn_raw_buffer_store_b128(v4u{0u, 0u, 0u, 0u}, rs, ST_OFF + (s0 + tid) * 16, 0, 16);

  const int ct0 = w, ct1 = w < 7 ? w + 8 : 7;
  const unsigned trl = ((g >> 1) * 16 + (g & 1) * 4 + q4) * 160 + p4 * 8;
  constexpr unsigned STAT = 20480, OIM = 21504, QKV = 40960;
  f32x4 acc1[2][5];
  {
    const unsigned wa0 = 10240 + trl + (ct0 / 5) * 10240 + (ct0 % 5) * 32, wa1 = 10240 + trl + (ct1 / 5) * 10240 + (ct1 % 5) * 32;
    const unsigned xa0 = r16 * 128 + ((g ^ ((r16 >> 1) & 7)) * 16), xa1 = xa0 ^ 64u;
#pragma unroll
    for (int i = 0; i < 2; ++i)
#pragma unroll
      for (int j = 0; j < 5; ++j) acc1[i][j] = f32x4{0.f, 0.f, 0.f, 0.f};
    float sm0 = 0.f, sq0 = 0.f, sm1 = 0.f, sq1 = 0.f;
    for (int s = 0; s < 10; ++s) {
      WAITV(10);
      BAR();
      if (s == 0) STAMP(2);
      const unsigned sb = (s & 3) * STAGE;
      v4u a0[2], a1[2], xb[2][5];
      const v4u sx = lds128(sb + tid * 16);
      const v4u sy = lds128(sb + 8192 + (tid & 127) * 16);
#pragma unroll
      for (int kk = 0; kk < 2; ++kk) {
        a0[kk] = cat(ldstr(sb + wa0 + kk * 5120), ldstr(sb + wa0 + kk * 5120 + 1280));
        a1[kk] = cat(ldstr(sb + wa1 + kk * 5120), ldstr(sb + wa1 + kk * 5120 + 1280));
#pragma unroll
        for (int j = 0; j < 5; ++j) xb[kk][j] = lds128(sb + (kk ? xa1 : xa0) + j * 2048);
      }
      SCHED();
#pragma unroll
      for (int e = 0; e < 4; ++e) {
        sm0 = dot2(sx[e], ONES, sm0); sq0 = dot2(sx[e], sx[e], sq0);
        sm1 = dot2(sy[e], ONES, sm1); sq1 = dot2(sy[e], sy[e], sq1);
      }
      if (s == 9) {
        sm0 = red8(sm0); sq0 = red8(sq0); sm1 = red8(sm1); sq1 = red8(sq1);
        const float m0_ = sm0 * (1.f / D_), v0_ = fmaxf(sq0 * (1.f / D_) - m0_ * m0_, 0.f);
        const float m1_ = sm1 * (1.f / D_), v1_ = fmaxf(sq1 * (1.f / D_) - m1_ * m1_, 0.f);
        if ((lane & 7) == 0) {
          sts64f(STAT + (tid >> 3) * 8, f32x2{m0_, __builtin_amdgcn_rsqf(v0_ + EPS)});
          if (w < 2) sts64f(STAT + (64 + (tid >> 3)) * 8, f32x2{m1_, __builtin_amdgcn_rsqf(v1_ + EPS)});
        }
      }
#pragma unroll
      for (int kk = 0; kk < 2; ++kk) {
#pragma unroll
        for (int e = 0; e < 4; ++e) xb[kk][0][e] = r16 == 0 ? ONES : xb[kk][0][e];
#pragma unroll
        for (int j = 4; j >= 0; --j) {
          acc1[0][j] = mfma(a0[kk], xb[kk][j], acc1[0][j]);
          acc1[1][j] = mfma(a1[kk], xb[kk][j], acc1[1][j]);
        }
      }
      SCHED();
      if (s < 7) issue(s + 3); else issue_wo(s - 7);
    }
  }
  STAMP(3);
  BAR();
  {
    f32x2 mr[5];
#pragma unroll
    for (int j = 0; j < 5; ++j) mr[j] = lds64f(STAT + (j * 16 + r16) * 8);
    SCHED();
#pragma unroll
    for (int i = 0; i < 2; ++i) {
      const int ct = i ? ct1 : ct0;
      f32x4 cs;
#pragma unroll
      for (int e = 0; e < 4; ++e) cs[e] = dpp<0x150>(acc1[i][0][e]);
      const unsigned dst = QKV + r16 * QP + ct * 32 + g * 8;
#pragma unroll
      for (int j = 0; j < 5; ++j) {
        const f32x4 a = acc1[i][j];
        const float m = mr[j].x, r = mr[j].y;
        sts64(dst + j * 16 * QP, v2u{pk((a.x - m * cs.x) * r, (a.y - m * cs.y) * r), pk((a.z - m * cs.z) * r, (a.w - m * cs.w) * r)});
      }
    }
  }
  BAR();
  STAMP(4);
  {
    const int hd = w & 3, par = w >> 2;
    const unsigned hb = QKV + par * QP + hd * 40;
    v4u kf[3], qf[2];
    v2u vt[3][2];
#pragma unroll
    for (int t = 0; t < 3; ++t) {
      const int i = t == 2 ? (r16 < 8 ? 32 + r16 : 39) : t * 16 + r16;
      const unsigned a = hb + 160 + i * 2 * QP + g * 16;
      kf[t] = cat(lds64(a), lds64(a + 8));
    }
#pragma unroll
    for (int t = 0; t < 2; ++t) {
      const unsigned a = hb + (8 + t * 16 + r16) * 2 * QP + g * 16;
      qf[t] = cat(lds64(a), lds64(a + 8));
    }
#pragma unroll
    for (int t = 0; t < 3; ++t) {
      const int i = t == 2 ? (g < 2 ? 32 + 4 * g + q4 : 39) : t * 16 + 4 * g + q4;
      const unsigned a = hb + 320 + i * 2 * QP + p4 * 8;
      vt[t][0] = ldstr(a); vt[t][1] = ldstr(a + 32);
    }
    f32x4 ba, bb;
#pragma unroll
    for (int r = 0; r < 4; ++r) {
      const int d = r16 - 4 * g - r;
      ba[r] = (d + 8 >= 0 && d + 8 <= 7) ? 0.f : -1e30f;
      bb[r] = (d - 8 >= 0 && d - 8 <= 7) ? 0.f : -1e30f;
    }
#pragma unroll
    for (int t = 0; t < 2; ++t) {
      v4u q = qf[t];
      q.x = g < 3 ? q.x : 0u; q.y = g < 3 ? q.y : 0u; q.z = g < 2 ? q.z : 0u; q.w = g < 2 ? q.w : 0u;
      f32x4 b0 = ba;
      if (t == 0) {
#pragma unroll
        for (int r = 0; r < 4; ++r) b0[r] = (st == 0 && g < 2) ? -1e30f : ba[r];
      }
      const f32x4 sa = mfma(kf[t], q, b0), sb_ = mfma(kf[t + 1], q, bb);
      float mx = fmaxf(fmaxf(fmaxf(sa.x, sa.y), fmaxf(sa.z, sa.w)), fmaxf(fmaxf(sb_.x, sb_.y), fmaxf(sb_.z, sb_.w)));
      mx = rows_max(mx);
      constexpr float C = 0.22360679774997896f * 1.4426950408889634f;
      const float mc = mx * C;
      float p[8];
#pragma unroll
      for (int r = 0; r < 4; ++r) { p[r] = __builtin_amdgcn_exp2f(fmaf(sa[r], C, -mc)); p[4 + r] = __builtin_amdgcn_exp2f(fmaf(sb_[r], C, -mc)); }
      const v4u pf = v4u{pk(p[0], p[1]), pk(p[2], p[3]), pk(p[4], p[5]), pk(p[6], p[7])};
      float sum = 0.f;
#pragma unroll
      for (int e = 0; e < 4; ++e) sum = dot2(pf[e], ONES, sum);
      const float inv = __builtin_amdgcn_rcpf(rows_sum(sum));
      const f32x4 z = f32x4{0.f, 0.f, 0.f, 0.f};
      const f32x4 o0 = mfma(cat(vt[t][0], vt[t + 1][0]), pf, z), o1 = mfma(cat(vt[t][1], vt[t + 1][1]), pf, z);
      const unsigned oa = OIM + (32 * t + 2 * r16 + par) * OP + hd * 40;
      sts64(oa + g * 8, v2u{pk(o0.x * inv, o0.y * inv), pk(o0.z * inv, o0.w * inv)});
      if (g == 0) sts64(oa + 32, v2u{pk(o1.x * inv, o1.y * inv), pk(o1.z * inv, o1.w * inv)});
    }
  }
  BAR();
  STAMP(5);
  const int tau = 8 * (r16 & 7) + 4 * (r16 >> 3);
  {
#pragma unroll
    for (int pass = 0; pass < 2; ++pass) {
      if (pass == 0 || w < 2) {
        const int ci = pass * 8 + w, c8 = dt * 10 + ci;
        const v4u v = lds128(OIM + (tau + g) * OP + ci * 16);
        __builtin_amdgcn_raw_buffer_store_b128(v, rs, OF_OFF + ((st * 4 + g) * 20 + (c8 >> 2)) * 1024 + ((c8 & 3) * 16 + r16) * 16, 0, 16);
      }
    }
  }
  WAITV(0);
  PIN(ep);
  const unsigned tag = __builtin_amdgcn_readfirstlane(ep) + 1u;
  BAR();
  if (tid == 0) __builtin_amdgcn_raw_buffer_store_b32(tag, rs, F1_OFF + st * 128 + dt * 4, 0, 16);
  STAMP(6);

  const bool p3 = idx < 30;
  const int i3 = p3 ? idx : idx - 2;
  const int d3 = (xcd & 1) * 5 + i3 % 5, f3 = (xcd >> 1) * 6 + i3 / 5;
  const int rot3 = (i3 % 5 + i3 / 5) & 7;
  v4u wr[8][4];
  const unsigned w1v = (8 * g + 4 * (r16 >> 3)) * 3072u + (r16 & 7) * 16;
  cptr w1b = w1 + (size_t)w * (256 * 3072) + f3 * 128;
  auto ldw1 = [&](int n) { gld16(wr[n >> 2][n & 3], w1v, w1b + (size_t)((((n >> 2) + rot3) & 7) * 32 + (n & 3)) * 3072); };
  const int ej = lane >> 5, ehalf = (lane >> 4) & 1;
  const int etok = tau + 2 * (w & 1) + ej;
  v4u hres[2];
  {
    const unsigned xo = (s0 + etok) * 1280 + dt * 160 + ehalf * 16;
    gld16(hres[0], xo + (w >> 1) * 32, x);
    gld16(hres[1], xo + 128, x);
  }
#pragma unroll
  for (int n = 0; n < N1A; ++n) ldw1(n);
  {
    const int kh = w >> 2, tj = w & 3;
    wait_flags(ws, F1_OFF + st * 128 + (kh * 4 + (lane & 3)) * 4, tag);
    STAMP(7);
    cptr ob = ws + OF_OFF + (size_t)((st * 4 + tj) * 20 + kh * 10) * 1024;
    v4u of[10];
#pragma unroll
    for (int j = 0; j < 10; ++j) {
      int kq = j + rot2;
      kq = kq >= 10 ? kq - 10 : kq;
      gld16c<0>(of[j], lo, ob + kq * 1024);
    }
    f32x4 acc[5];
#pragma unroll
    for (int c = 0; c < 5; ++c) acc[c] = f32x4{0.f, 0.f, 0.f, 0.f};
    const unsigned wl = trl + kh * 5120;
    v4u af[2][5];
#pragma unroll
    for (int c = 0; c < 5; ++c) { const unsigned a = 81920 + wl + c * 32; af[0][c] = cat(ldstr(a), ldstr(a + 1280)); }
#pragma unroll
    for (int j = 0; j < 10; ++j) {
      if (j < 9) {
#pragma unroll
        for (int c = 0; c < 5; ++c) {
          const unsigned a = (j + 1 < 8 ? 81920 + (j + 1) * 10240 : (j + 1 - 8) * 10240) + wl + c * 32;
          af[(j + 1) & 1][c] = cat(ldstr(a), ldstr(a + 1280));
        }
      }
      WAITV(9 - j + (j < N1B ? j : N1B));
      PIN(of[j]);
#pragma unroll
      for (int c = 0; c < 5; ++c) acc[c] = mfma(af[j & 1][c], of[j], acc[c]);
      SCHED();
      if (j < N1B) ldw1(N1A + j);
    }
    STAMP(8);
    constexpr unsigned RED = 40960, HIM = 21504, PST = 32768;
#pragma unroll
    for (int c = 0; c < 5; ++c) sts128f(RED + ((kh * 5 + c) * 4 + tj) * 1024 + lo, acc[c]);
    BAR();
    PIN(hres[0]); PIN(hres[1]);
#pragma unroll
    for (int pass = 0; pass < 2; ++pass) {
      if (pass == 0 || w < 2) {
        const int P = pass * 8 + w, ct = P >> 1;
        const unsigned ra = RED + (ct * 4 + 2 * (w & 1) + ej) * 1024 + (ehalf * 32 + r16) * 16;
        const f32x4 a0 = lds128f(ra) + lds128f(ra + 20480), a1 = lds128f(ra + 256) + lds128f(ra + 256 + 20480);
        const v4u xv = hres[pass];
        v4u h;
        h.x = pk(a0.x + blo(xv.x), a0.y + bhi(xv.x)); h.y = pk(a0.z + blo(xv.y), a0.w + bhi(xv.y));
        h.z = pk(a1.x + blo(xv.z), a1.y + bhi(xv.z)); h.w = pk(a1.z + blo(xv.w), a1.w + bhi(xv.w));
        hres[pass] = h;
        sts128(HIM + swap23(etok) * 160 + ct * 32 + ehalf * 16, h);
        float sm = 0.f, sq = 0.f;
#pragma unroll
        for (int e = 0; e < 4; ++e) { sm = dot2(h[e], ONES, sm); sq = dot2(h[e], h[e], sq); }
        sts64f(PST + (etok * 10 + ct * 2 + ehalf) * 8, f32x2{sm, sq});
      }
    }
    BAR();
#pragma unroll
    for (int pass = 0; pass < 2; ++pass) {
      if (pass == 0 || w < 2) {
        const int P = pass * 8 + w, ct = P >> 1, ks = P & 1;
        const unsigned a = HIM + ks * 5120 + trl + ct * 32;
        const v4u v = cat(ldstr(a), ldstr(a + 1280));
        __builtin_amdgcn_raw_buffer_store_b128(v, rs, HF_OFF + ((dt * 5 + ct) * 64 + st * 2 + ks) * 1024 + lo, 0, 16);
      }
    }
    if (w >= 6) {
      float s = 0.f;
#pragma unroll
      for (int i = 0; i < 10; ++i) s += lds32f(PST + (lane * 10 + i) * 8 + (w & 1) * 4);
      __hip_atomic_fetch_add((GAS double*)(wsp + ST_OFF) + (s0 + lane) * 2 + (w & 1), (double)s, __ATOMIC_RELAXED, __HIP_MEMORY_SCOPE_AGENT);
    }
    STAMP(9);
    WAITV(0);
    BAR();
    if (tid == 0) __builtin_amdgcn_raw_buffer_store_b32(tag, rs, F2_OFF + (st * 8 + dt) * 4, 0, 16);
    STAMP(10);
  }

  v4u vr[6][4];
  const int rot4 = (idx & 7) % 6, rot5 = (idx >> 3);
  unsigned w2v;
#define RELANE() do { lane = threadIdx.x & 63; PIN(lane); g = lane >> 4; r16 = lane & 15; lo = lane * 16; } while (0)
#define MKW2V() w2v = (32 * (g & 1) + 4 * (g >> 1) + (r16 >> 3)) * 4096u + (r16 & 7) * 16
  cptr w2b = w2 + (size_t)w * (192 * 4096) + st * 128;
  auto ldw2 = [&](int n) {
    int kk = (n >> 2) + rot4;
    kk = kk >= 6 ? kk - 6 : kk;
    gld16(vr[n >> 2][n & 3], w2v, w2b + (size_t)((kk >> 1) * 64 + (kk & 1) * 2 + (n & 3) * 8) * 4096);
  };
  if (p3) {
    constexpr unsigned PARK = 147456;
    v4u bias;
    {
      const int fb = f3 * 64 + 32 * (g & 1) + 4 * (g >> 1) + 2 * (w >> 2);
#pragma unroll
      for (int r = 0; r < 4; ++r) { unsigned t_; gld4(t_, (fb + 8 * r) * 2, b1); bias[r] = t_; }
    }
#pragma unroll
    for (int n = N1A + N1B; n < 32; ++n) ldw1(n);
    sts128(PARK + w * 1024 + lo, hres[0]);
    if (w < 2) sts128(PARK + 8192 + w * 1024 + lo, hres[1]);
#pragma unroll
    for (int kk = 0; kk < 8; ++kk) {
      if (4 * kk + 4 > N1A + N1B) WAITV(32 - (4 * kk + 4));
#pragma unroll
      for (int j = 0; j < 4; ++j) PIN(wr[kk][j]);
      xpose(wr[kk]);
#pragma unroll
      for (int j = 0; j < 4; ++j) PIN(wr[kk][j]);
    }
    PIN(bias);
    if (r16 == 0) sts128(PARK + 10240 + (w * 4 + g) * 16, bias);
    STAMP(11);
    wait_flags(ws, F2_OFF + w * 128 + (lane & 31) * 4, tag);
    STAMP(12);
    RELANE();
    constexpr int HR = 10;
    v4u sv[4], hf[HR];
    {
      cptr sp = ws + ST_OFF + (size_t)w * 4096;
      gld16c<0>(sv[0], lane * 64, sp); gld16c<16>(sv[1], lane * 64, sp); gld16c<32>(sv[2], lane * 64, sp); gld16c<48>(sv[3], lane * 64, sp);
    }
    cptr hb = ws + HF_OFF + (size_t)(d3 * 4 * 64 + w * 8) * 1024;
    auto ldh = [&](int n) {
      gld16c<0>(hf[n % HR], lo, hb + (size_t)((n >> 4) * 2 + (n & 1)) * 65536 + ((((n >> 1) & 7) + rot3) & 7) * 1024);
    };
#pragma unroll
    for (int n = 0; n < HR; ++n) ldh(n);
    const unsigned SA = 131072 + w * 2048, SB = SA + 1024;
    WAITV(HR);
    {
      f32x4 a, c;
#pragma unroll
      for (int i = 0; i < 4; ++i) {
        PIN(sv[i]);
        const double sm = __builtin_bit_cast(double, v2u{sv[i].x, sv[i].y}), sq = __builtin_bit_cast(double, v2u{sv[i].z, sv[i].w});
        const double md = sm * (1.0 / D_);
        const float mean = (float)md, var = fmaxf((float)(sq * (1.0 / D_) - md * md), 0.f);
        const float r1 = __builtin_amdgcn_rsqf(var + EPS), r2 = __builtin_amdgcn_rsqf(var * r1 * r1 + EPS);
        a[i] = r1 * r2; c[i] = -mean * r1 * r2;
      }
      sts128f(SA + lo, a);
      sts128f(SB + lo, c);
    }
    STAMP(13);
#pragma unroll
    for (int pass = 0; pass < 2; ++pass) {
      f32x4 acc[4][2];
#pragma unroll
      for (int i = 0; i < 4; ++i) { acc[i][0] = f32x4{0.f, 0.f, 0.f, 0.f}; acc[i][1] = acc[i][0]; }
#pragma unroll
      for (int kk = 0; kk < 8; ++kk) {
        const int t = pass * 8 + kk;
        const unsigned so = ((kk + rot3) & 7) * 128 + g * 32;
        const f32x4 a0 = lds128f(SA + so), a1 = lds128f(SA + so + 16), c0 = lds128f(SB + so), c1 = lds128f(SB + so + 16);
        WAITV(30 - 2 * t < HR - 2 ? 30 - 2 * t : HR - 2);
        v4u hn[2];
#pragma unroll
        for (int j = 0; j < 2; ++j) {
          PIN(hf[(2 * t + j) % HR]);
          const v4u h = hf[(2 * t + j) % HR];
          hn[j].x = pk(fmaf(blo(h.x), a0.x, c0.x), fmaf(bhi(h.x), a0.y, c0.y));
          hn[j].y = pk(fmaf(blo(h.y), a0.z, c0.z), fmaf(bhi(h.y), a0.w, c0.w));
          hn[j].z = pk(fmaf(blo(h.z), a1.x, c1.x), fmaf(bhi(h.z), a1.y, c1.y));
          hn[j].w = pk(fmaf(blo(h.w), a1.z, c1.z), fmaf(bhi(h.w), a1.w, c1.w));
        }
        PIN(hn[0]); PIN(hn[1]);
        if (2 * t + HR < 32) { ldh(2 * t + HR); ldh(2 * t + HR + 1); }
#pragma unroll
        for (int i = 0; i < 4; ++i) { acc[i][0] = mfma(wr[kk][i], hn[0], acc[i][0]); acc[i][1] = mfma(wr[kk][i], hn[1], acc[i][1]); }
      }
#pragma unroll
      for (int i = 0; i < 4; ++i)
#pragma unroll
        for (int j = 0; j < 2; ++j) sts128f(w * 16384 + (i * 4 + pass * 2 + j) * 1024 + lo, acc[i][j]);
    }
    STAMP(14);
    RELANE(); MKW2V();
#pragma unroll
    for (int n = 0; n < N2A; ++n) ldw2(n);
    BAR();
    if (b == 0 && tid == 0) __builtin_amdgcn_raw_buffer_store_b32(tag, rs, EP_OFF, 0, 16);
    {
      const int dj = w & 3, kp = w >> 2;
      const unsigned ra = (2 * kp * 4 + dj) * 1024 + lo;
      f32x4 s0_ = f32x4{0.f, 0.f, 0.f, 0.f}, s1_ = s0_;
#pragma unroll
      for (int v = 0; v < 8; ++v) { s0_ += lds128f(ra + v * 16384); s1_ += lds128f(ra + v * 16384 + 4096); }
      bias = lds128(PARK + 10240 + (w * 4 + g) * 16);
      hres[0] = lds128(PARK + w * 1024 + lo);
      hres[1] = lds128(PARK + 8192 + (w & 1) * 1024 + lo);
      v4u u;
      u.x = pk(gelu_tanh(s0_.x + blo(bias[0])), gelu_tanh(s0_.y + blo(bias[1])));
      u.y = pk(gelu_tanh(s0_.z + blo(bias[2])), gelu_tanh(s0_.w + blo(bias[3])));
      u.z = pk(gelu_tanh(s1_.x + bhi(bias[0])), gelu_tanh(s1_.y + bhi(bias[1])));
      u.w = pk(gelu_tanh(s1_.z + bhi(bias[2])), gelu_tanh(s1_.w + bhi(bias[3])));
      __builtin_amdgcn_raw_buffer_store_b128(u, rs, UF_OFF + ((d3 * 4 + dj) * 48 + f3 * 2 + kp) * 1024 + lo, 0, 16);
    }
    STAMP(15);
    WAITV(0);
    BAR();
    if (tid == 0) __builtin_amdgcn_raw_buffer_store_b32(tag, rs, F3_OFF + d3 * 128 + f3 * 4, 0, 16);
    STAMP(16);
  } else {
    RELANE(); MKW2V();
#pragma unroll
    for (int n = 0; n < N2A; ++n) ldw2(n);
    WAITV(0);
  }

  {
#pragma unroll
    for (int n = N2A; n < 24; ++n) ldw2(n);
    const int ej = lane >> 5, ehalf = (lane >> 4) & 1;
    const int etok = 8 * (r16 & 7) + 4 * (r16 >> 3) + 2 * (w & 1) + ej;
    unsigned bq;
    asm volatile("global_load_ushort %0, %1, %2" : "=v"(bq) : "v"((s0 + etok) * 2), "s"(b2) : "memory");
#pragma unroll
    for (int kk = 0; kk < 6; ++kk) {
      WAITV(1 + 24 - (4 * kk + 4));
#pragma unroll
      for (int j = 0; j < 4; ++j) PIN(vr[kk][j]);
      xpose(vr[kk]);
#pragma unroll
      for (int j = 0; j < 4; ++j) PIN(vr[kk][j]);
    }
    STAMP(17);
    {
      const int l6 = lane % 6;
      wait_flags(ws, F3_OFF + ((dt * 80) / 64 + (l6 & 1)) * 128 + (3 * w + (l6 >> 1)) * 4, tag);
    }
    STAMP(18);
    constexpr int UR = 18;
    v4u uf[UR];
    cptr ub = ws + UF_OFF + (size_t)(dt * 5 * 48 + w * 6) * 1024;
    auto ldu = [&](int n) {
      int c = n / 6 + rot5, kk = n % 6 + rot4;
      c = c >= 5 ? c - 5 : c;
      kk = kk >= 6 ? kk - 6 : kk;
      gld16c<0>(uf[n % UR], lo, ub + (size_t)c * 49152 + kk * 1024);
    };
#pragma unroll
    for (int n = 0; n < UR; ++n) ldu(n);
#pragma unroll
    for (int c = 0; c < 5; ++c) {
      f32x4 acc[4];
#pragma unroll
      for (int j = 0; j < 4; ++j) acc[j] = f32x4{0.f, 0.f, 0.f, 0.f};
#pragma unroll
      for (int kk = 0; kk < 6; ++kk) {
        const int n = c * 6 + kk;
        WAITV(29 - n < UR - 1 ? 29 - n : UR - 1);
        PIN(uf[n % UR]);
#pragma unroll
        for (int j = 0; j < 4; ++j) acc[j] = mfma(uf[n % UR], vr[kk][j], acc[j]);
        SCHED();
        if (n + UR < 30) ldu(n + UR);
      }
      int cr = c + rot5;
      cr = cr >= 5 ? cr - 5 : cr;
#pragma unroll
      for (int j = 0; j < 4; ++j) sts128f(w * 20480 + (cr * 4 + j) * 1024 + lo, acc[j]);
    }
    STAMP(19);
    BAR();
    PIN(bq);
    const float bias = blo(bq);
#pragma unroll
    for (int pass = 0; pass < 2; ++pass) {
      if (pass == 0 || w < 2) {
        const int P = pass * 8 + w, ct = P >> 1;
        const unsigned ra = (ct * 4 + 2 * (w & 1) + ej) * 1024 + (ehalf * 32 + r16) * 16;
        f32x4 s0_ = f32x4{0.f, 0.f, 0.f, 0.f}, s1_ = s0_;
#pragma unroll
        for (int v = 0; v < 8; ++v) { s0_ += lds128f(ra + v * 20480); s1_ += lds128f(ra + v * 20480 + 256); }
        const v4u hv = hres[pass];
        v4u o;
        o.x = pk(s0_.x + bias + blo(hv.x), s0_.y + bias + bhi(hv.x)); o.y = pk(s0_.z + bias + blo(hv.y), s0_.w + bias + bhi(hv.y));
        o.z = pk(s1_.x + bias + blo(hv.z), s1_.y + bias + bhi(hv.z)); o.w = pk(s1_.z + bias + blo(hv.w), s1_.w + bias + bhi(hv.w));
        *(GAS v4u*)(out + (size_t)(s0 + etok) * 1280 + dt * 160 + ct * 32 + ehalf * 16) = o;
      }
    }
    STAMP(20);
  }
}

extern "C" void kernel_launch(void* const* d_in, const int* in_sizes, int n_in, void* d_out, int out_size, void* d_ws,
                              size_t ws_size, hipStream_t stream) {
  (void)hipFuncSetAttribute((const void*)fused_block, hipFuncAttributeMaxDynamicSharedMemorySize, 163840);
  fused_block<<<256, 512, 163840, stream>>>((cptr)d_in[0], (cptr)d_in[1], (cptr)d_in[2], (cptr)d_in[4], (cptr)d_in[5], (cptr)d_in[6],
                                            (cptr)d_in[7], (unsigned char*)d_out, (unsigned char*)d_ws);
}
```

```cpp
#include <hip/hip_runtime.h>
#include <hip/hip_bf16.h>

typedef unsigned v4u __attribute__((ext_vector_type(4)));
typedef unsigned v2u __attribute__((ext_vector_type(2)));
typedef float f32x4 __attribute__((ext_vector_type(4)));
typedef float f32x2 __attribute__((ext_vector_type(2)));
typedef short v4s __attribute__((ext_vector_type(4)));
typedef __bf16 bf16x8 __attribute__((ext_vector_type(8)));
typedef __bf16 bf16x2 __attribute__((ext_vector_type(2)));
#define LAS __attribute__((address_space(3)))
#define GAS __attribute__((address_space(1)))
typedef const unsigned char* cptr;

constexpr int D_ = 640;
constexpr unsigned OF_OFF = 0;
constexpr unsigned HF_OFF = OF_OFF + 2621440;
constexpr unsigned UF_OFF = HF_OFF + 2 * 2621440;
constexpr unsigned GR_OFF = UF_OFF + 1966080;
constexpr unsigned F1_OFF = GR_OFF + 262144;
constexpr unsigned F2_OFF = F1_OFF + 4096;
constexpr unsigned F3_OFF = F2_OFF + 1024;
constexpr unsigned EP_OFF = F3_OFF + 2048;
constexpr unsigned WS_BYTES = EP_OFF + 256;
constexpr float EPS = 1e-5f;
constexpr unsigned SPIN_CAP = 1u << 16;

#define STAMP(k)

#define WAITV(n) asm volatile("s_waitcnt vmcnt(%0)" ::"i"(n) : "memory")
#define WAITL0() asm volatile("s_waitcnt lgkmcnt(0)" ::: "memory")
#define BAR() asm volatile("s_waitcnt lgkmcnt(0)\n\ts_barrier" ::: "memory")
#define PIN(x) asm volatile("" : "+v"(x))
#define SCHED() __builtin_amdgcn_sched_barrier(0)

__device__ __forceinline__ void dma16(unsigned voff, cptr base, unsigned ldsaddr) {
  asm volatile("s_mov_b32 m0, %2\n\ts_nop 0\n\tglobal_load_lds_dwordx4 %0, %1" ::"v"(voff), "s"(base), "s"(ldsaddr) : "memory");
}
__device__ __forceinline__ void dma16m(unsigned voff, cptr base, unsigned ldsaddr, unsigned long long mask) {
  asm volatile("s_mov_b32 m0, %2\n\ts_mov_b64 exec, %3\n\tglobal_load_lds_dwordx4 %0, %1\n\ts_mov_b64 exec, -1" ::"v"(voff), "s"(base), "s"(ldsaddr), "s"(mask) : "memory");
}
__device__ __forceinline__ void gld16(v4u& dst, unsigned voff, cptr base) {
  asm volatile("global_load_dwordx4 %0, %1, %2" : "=v"(dst) : "v"(voff), "s"(base) : "memory");
}
template <int OFF> __device__ __forceinline__ void gld16c(v4u& dst, unsigned voff, cptr base) {
  asm volatile("global_load_dwordx4 %0, %1, %2 offset:%3 sc1" : "=v"(dst) : "v"(voff), "s"(base), "i"(OFF) : "memory");
}
__device__ __forceinline__ void gld4(unsigned& dst, unsigned voff, cptr base) {
  asm volatile("global_load_dword %0, %1, %2" : "=v"(dst) : "v"(voff), "s"(base) : "memory");
}

__device__ __forceinline__ v4u lds128(unsigned a) { return *(const LAS v4u*)(size_t)a; }
__device__ __forceinline__ f32x4 lds128f(unsigned a) { return *(const LAS f32x4*)(size_t)a; }
__device__ __forceinline__ v2u lds64(unsigned a) { return *(const LAS v2u*)(size_t)a; }
__device__ __forceinline__ f32x2 lds64f(unsigned a) { return *(const LAS f32x2*)(size_t)a; }
__device__ __forceinline__ float lds32f(unsigned a) { return *(const LAS float*)(size_t)a; }
__device__ __forceinline__ void sts128(unsigned a, v4u v) { *(LAS v4u*)(size_t)a = v; }
__device__ __forceinline__ void sts128f(unsigned a, f32x4 v) { *(LAS f32x4*)(size_t)a = v; }
__device__ __forceinline__ void sts64(unsigned a, v2u v) { *(LAS v2u*)(size_t)a = v; }
__device__ __forceinline__ void sts64f(unsigned a, f32x2 v) { *(LAS f32x2*)(size_t)a = v; }
__device__ __forceinline__ void sts32f(unsigned a, float v) { *(LAS float*)(size_t)a = v; }
__device__ __forceinline__ v2u ldstr(unsigned a) {
  return __builtin_bit_cast(v2u, __builtin_amdgcn_ds_read_tr16_b64_v4i16((LAS v4s*)(size_t)a));
}
__device__ __forceinline__ v4u cat(v2u a, v2u b) { return v4u{a.x, a.y, b.x, b.y}; }
__device__ __forceinline__ f32x4 mfma(v4u a, v4u b, f32x4 c) {
  return __builtin_amdgcn_mfma_f32_16x16x32_bf16(__builtin_bit_cast(bf16x8, a), __builtin_bit_cast(bf16x8, b), c, 0, 0, 0);
}
__device__ __forceinline__ float dot2(unsigned a, unsigned b, float c) {
  return __builtin_amdgcn_fdot2_f32_bf16(__builtin_bit_cast(bf16x2, a), __builtin_bit_cast(bf16x2, b), c, false);
}
__device__ __forceinline__ unsigned pk(float lo, float hi) {
  return __builtin_bit_cast(unsigned, __builtin_convertvector(f32x2{lo, hi}, bf16x2));
}
__device__ __forceinline__ float blo(unsigned u) { return __builtin_bit_cast(float, u << 16); }
__device__ __forceinline__ float bhi(unsigned u) { return __builtin_bit_cast(float, u & 0xffff0000u); }
template <int CTRL> __device__ __forceinline__ float dpp(float v) {
  return __builtin_bit_cast(float, __builtin_amdgcn_mov_dpp(__builtin_bit_cast(int, v), CTRL, 0xF, 0xF, true));
}
__device__ __forceinline__ float red8(float v) {
  v += dpp<0xB1>(v); v += dpp<0x4E>(v); v += dpp<0x141>(v);
  return v;
}
__device__ __forceinline__ float asf(unsigned u) { return __builtin_bit_cast(float, u); }
__device__ __forceinline__ unsigned asu(float f) { return __builtin_bit_cast(unsigned, f); }
__device__ __forceinline__ float mx2(float a, float b) { return __builtin_elementwise_maximum(a, b); }
__device__ __forceinline__ float rows_max(float v) {
  const v2u a = __builtin_amdgcn_permlane16_swap(asu(v), asu(v), false, false);
  v = mx2(asf(a.x), asf(a.y));
  const v2u c = __builtin_amdgcn_permlane32_swap(asu(v), asu(v), false, false);
  return mx2(asf(c.x), asf(c.y));
}
__device__ __forceinline__ float rows_sum(float v) {
  const v2u a = __builtin_amdgcn_permlane16_swap(asu(v), asu(v), false, false);
  v = asf(a.x) + asf(a.y);
  const v2u c = __builtin_amdgcn_permlane32_swap(asu(v), asu(v), false, false);
  return asf(c.x) + asf(c.y);
}
__device__ __forceinline__ unsigned swap23(unsigned r) { return (r & ~12u) | ((r & 4u) << 1) | ((r & 8u) >> 1); }
constexpr unsigned ONES = 0x3f803f80u;

__device__ __forceinline__ void xpose(v4u (&r)[4]) {
  unsigned lo[4][2], hi[4][2];
#pragma unroll
  for (int j = 0; j < 4; ++j)
#pragma unroll
    for (int q = 0; q < 2; ++q) {
      lo[j][q] = __builtin_amdgcn_update_dpp(r[j][q], r[j][2 + q], 0x128, 0xF, 0xC, false);
      hi[j][q] = __builtin_amdgcn_update_dpp(r[j][2 + q], r[j][q], 0x128, 0xF, 0x3, false);
    }
#pragma unroll
  for (int i = 0; i < 4; ++i) {
    const unsigned sel = (i & 1) ? 0x07060302u : 0x05040100u;
    r[i] = v4u{__builtin_amdgcn_perm(lo[1][i >> 1], lo[0][i >> 1], sel), __builtin_amdgcn_perm(lo[3][i >> 1], lo[2][i >> 1], sel),
               __builtin_amdgcn_perm(hi[1][i >> 1], hi[0][i >> 1], sel), __builtin_amdgcn_perm(hi[3][i >> 1], hi[2][i >> 1], sel)};
  }
}

__device__ __forceinline__ void wait_flags(cptr ws, unsigned voff, unsigned tag) {
#pragma unroll 1
  for (unsigned spin = 0; spin < SPIN_CAP; ++spin) {
    unsigned v;
    asm volatile("global_load_dword %0, %1, %2 sc1\n\ts_waitcnt vmcnt(0)" : "=v"(v) : "v"(voff), "s"(ws) : "memory");
    if (__builtin_amdgcn_ballot_w64(v != tag) == 0ull) break;
    __builtin_amdgcn_s_sleep(2);
  }
}
__device__ __forceinline__ float gelu_tanh(float v) {
  const float z = v * (-2.3022081986f - 0.10294324f * v * v);
  return v * __builtin_amdgcn_rcpf(1.f + __builtin_amdgcn_exp2f(z));
}

constexpr int STAGE = 40960;
constexpr int QP = 488, OP = 176;
constexpr int N1Z = 0, N1A = 6, N1B = 10;
constexpr int N2A = 8;

__global__ __launch_bounds__(512, 2) void fused_block(cptr x, cptr wqkv, cptr wo, cptr w1, cptr b1, cptr w2, cptr b2,
                                                      unsigned char* out, unsigned char* wsp) {
  extern __shared__ __attribute__((aligned(16))) unsigned char smem[];
  cptr ws = wsp;
  const __amdgpu_buffer_rsrc_t rs = __builtin_amdgcn_make_buffer_rsrc((void*)wsp, 0, (int)WS_BYTES, 0x00020000);
  const int tid = threadIdx.x, w = __builtin_amdgcn_readfirstlane(tid >> 6);
  int lane = tid & 63;
  const int b = blockIdx.x, xcd = b & 7, idx = b >> 3;
  const int st = (xcd >> 1) * 8 + (idx & 7), dt = (xcd & 1) * 4 + (idx >> 3);
  const int s0 = st * 64;
  int g = lane >> 4, r16 = lane & 15, q4 = (lane & 15) >> 2, p4 = lane & 3;
  unsigned lo = lane * 16;
  STAMP(0);

  unsigned ep;
  asm volatile("global_load_dword %0, %1, %2 sc1" : "=v"(ep) : "v"(EP_OFF), "s"(ws) : "memory");
  unsigned junk = 0;
  {
    unsigned long long pc; unsigned len;
    asm volatile("s_getpc_b64 %0\n.Lfb_here:\n\ts_mov_b32 %1, .Lfb_end-.Lfb_here" : "=s"(pc), "=s"(len));
    unsigned off = (idx * 16 + (lane & 15)) * 128;
    off = off < len ? off : 0;
    if (w == 7) asm volatile("global_load_dword %0, %1, %2" : "+v"(junk) : "v"(off), "s"(pc) : "memory");
  }

  const int rot1 = ((idx & 7) + 3 * (idx >> 3)) % 10;
  unsigned voff[5];
#pragma unroll
  for (int i = 0; i < 5; ++i) {
    const int id = w + 8 * i;
    if (i == 0 || (i == 1 && w < 2)) {
      const int row = 8 * id + (lane >> 3), ch = (lane & 7) ^ ((row >> 1) & 7);
      int tok = s0 - 16 + row;
      tok = tok < 0 ? 0 : tok;
      voff[i] = tok * 1280 + ch * 16;
    } else {
      const unsigned c = (id - 10) * 64 + lane, R = (c * 6554u) >> 16, ch = c - R * 10u;
      voff[i] = swap23(R & 63u) * 3840u + (R >> 6) * 1280u + dt * 160 + ch * 16;
    }
  }
  const unsigned long long xmask = w == 0 ? ~0xFFull : ~0ull;
  auto issue1 = [&](int s, int i) {
    int sr = s + rot1;
    sr = sr >= 10 ? sr - 10 : sr;
    cptr bx = x + sr * 128, bw = wqkv + (size_t)sr * (64 * 3840);
    if (i == 0) dma16m(voff[0], bx, (s & 3) * STAGE + w * 1024, xmask);
    else dma16(voff[i], (i == 1 && w < 2) ? bx : bw, (s & 3) * STAGE + w * 1024 + i * 8192);
  };
  auto issue = [&](int s) {
#pragma unroll
    for (int i = 0; i < 5; ++i) issue1(s, i);
  };
  issue(0);
  asm volatile("s_barrier" ::: "memory");
  issue(1);
  STAMP(1);
  const int rot2 = idx & 7;
  unsigned wov[5];
#pragma unroll
  for (int i = 0; i < 5; ++i) {
    const unsigned c = i * 64 + lane, R = (c * 6554u) >> 16, ch = c - R * 10u;
    wov[i] = swap23(R) * 1280u + dt * 160 + ch * 16;
  }
  auto issue_wo1 = [&](int grp, int i) {
    const int q = grp * 8 + w;
    int kq = (q >> 1) + rot2;
    kq = kq >= 10 ? kq - 10 : kq;
    dma16(wov[i], wo + ((q & 1) * 10 + kq) * (32 * 1280), (grp < 2 ? 81920 + q * 5120 : w * 5120) + i * 1024);
  };
  if (tid < 32) sts128((tid >> 3) * STAGE + (tid & 7) * 16, v4u{ONES, ONES, ONES, ONES});

  const bool p3 = idx < 30;
  const int i3 = p3 ? idx : idx - 2;
  const int d3 = (xcd & 1) * 5 + i3 % 5, f3 = (xcd >> 1) * 6 + i3 / 5;
  const int rot3 = (i3 % 5 + i3 / 5) & 7, rot3d = (i3 / 5) & 3;
  v4u wr[8][4];
  const unsigned w1v = (8 * g + 4 * (r16 >> 3)) * 3072u + (r16 & 7) * 16;
  cptr w1b = w1 + (size_t)w * (256 * 3072) + f3 * 128;
  auto ldw1 = [&](int n) { gld16(wr[n >> 2][n & 3], w1v, w1b + (size_t)((((n >> 2) + rot3) & 7) * 32 + (n & 3)) * 3072); };

  const int ct0 = w, ct1 = w < 7 ? w + 8 : 7;
  const unsigned trl = ((g >> 1) * 16 + (g & 1) * 4 + q4) * 160 + p4 * 8;
  constexpr unsigned STAT = 20480, OIM = 21504, QKV = 40960;
  f32x4 acc1[2][5];
  {
    const unsigned wa0 = 10240 + trl + (ct0 / 5) * 10240 + (ct0 % 5) * 32, wa1 = 10240 + trl + (ct1 / 5) * 10240 + (ct1 % 5) * 32;
    const unsigned xa0 = r16 * 128 + ((g ^ ((r16 >> 1) & 7)) * 16), xa1 = xa0 ^ 64u;
#pragma unroll
    for (int i = 0; i < 2; ++i)
#pragma unroll
      for (int j = 0; j < 5; ++j) acc1[i][j] = f32x4{0.f, 0.f, 0.f, 0.f};
    v4u a0[2], a1[2], xb[2][5], sx[2];
    f32x4 gram = f32x4{0.f, 0.f, 0.f, 0.f};
    const int srow = r16 < 10 ? 10 * w + r16 : 0;
    const unsigned sa0 = srow * 128 + ((g ^ ((srow >> 1) & 7)) * 16);
    auto half = [&](int kc, bool rdn, unsigned sb, int dma, int s) {
      const int kn = kc ^ 1;
      const unsigned xa = sb + (kn ? xa1 : xa0), ta0 = sb + wa0 + kn * 5120, ta1 = sb + wa1 + kn * 5120;
      v2u t0, t1;
#pragma unroll
      for (int j = 4; j >= 0; --j) {
        if (rdn) {
          if (j == 4) { xb[kn][4] = lds128(xa + 8192); t0 = ldstr(ta0); }
          if (j == 3) { a0[kn] = cat(t0, ldstr(ta0 + 1280)); t1 = ldstr(ta1); }
          if (j == 2) { a1[kn] = cat(t1, ldstr(ta1 + 1280)); xb[kn][3] = lds128(xa + 6144); }
          if (j == 1) { xb[kn][2] = lds128(xa + 4096); xb[kn][1] = lds128(xa + 2048); }
          if (j == 0) { xb[kn][0] = lds128(xa); sx[kn] = lds128((sb + sa0) ^ (kn * 64)); }
        }
        acc1[0][j] = mfma(a0[kc], xb[kc][j], acc1[0][j]);
        SCHED();
        acc1[1][j] = mfma(a1[kc], xb[kc][j], acc1[1][j]);
        if (j == 0 && rdn) gram = mfma(sx[kc], sx[kc], gram);
        SCHED();
        if (dma == 1) issue1(s + 3, 4 - j);
        if (dma == 2) issue_wo1(0, 4 - j);
      }
    };
    WAITV(5);
    PIN(junk);
    BAR();
    STAMP(2);
    {
#pragma unroll
      for (int j = 4; j >= 0; --j) xb[0][j] = lds128(xa0 + j * 2048);
      a0[0] = cat(ldstr(wa0), ldstr(wa0 + 1280)); a1[0] = cat(ldstr(wa1), ldstr(wa1 + 1280));
      sx[0] = lds128(sa0);
    }
    issue(2);
#pragma unroll 1
    for (int s = 0; s < 7; ++s) {
      half(0, true, (s & 3) * STAGE, 0, s);
      WAITV(5); BAR();
      half(1, true, ((s + 1) & 3) * STAGE, 1, s);
    }
    half(0, true, 3 * STAGE, 0, 7);
    WAITV(5); BAR();
    half(1, true, 0, 0, 7);
    half(0, true, 0, 0, 8);
    WAITV(0); BAR();
    half(1, true, STAGE, 2, 8);
    half(0, true, STAGE, 0, 9);
    gram = mfma(sx[1], sx[1], gram);
    half(1, false, 0, 0, 9);
    {
      const float dg = p4 == 0 ? gram.x : p4 == 1 ? gram.y : p4 == 2 ? gram.z : gram.w;
      const float sq = rows_sum(g == q4 ? dg : 0.f), sm = rows_sum(g == 3 ? gram.w : 0.f);
      const float m_ = sm * (1.f / D_), v_ = fmaxf(sq * (1.f / D_) - m_ * m_, 0.f);
      if (lane < 10) sts64f(STAT + srow * 8, f32x2{m_, __builtin_amdgcn_rsqf(v_ + EPS)});
    }
    BAR();
  }
  STAMP(3);
  {
    f32x2 mr[5];
#pragma unroll
    for (int j = 0; j < 5; ++j) mr[j] = lds64f(STAT + (j * 16 + r16) * 8);
    SCHED();
#pragma unroll
    for (int i = 0; i < 2; ++i) {
      const int ct = i ? ct1 : ct0;
      f32x4 cs;
#pragma unroll
      for (int e = 0; e < 4; ++e) cs[e] = dpp<0x150>(acc1[i][0][e]);
      const unsigned dst = QKV + r16 * QP + ct * 32 + g * 8;
#pragma unroll
      for (int j = 0; j < 5; ++j) {
        const f32x4 a = acc1[i][j];
        const float m = mr[j].x, r = mr[j].y;
        sts64(dst + j * 16 * QP, v2u{pk((a.x - m * cs.x) * r, (a.y - m * cs.y) * r), pk((a.z - m * cs.z) * r, (a.w - m * cs.w) * r)});
      }
    }
  }
#pragma unroll
  for (int i = 0; i < 5; ++i) issue_wo1(1, i);
  if (w < 4) {
#pragma unroll
    for (int i = 0; i < 5; ++i) issue_wo1(2, i);
  }
#pragma unroll
  for (int n = 0; n < N1Z; ++n) ldw1(n);
  {
    const int hd = w & 3, par = w >> 2;
    const unsigned hb = QKV + par * QP + hd * 40;
    unsigned ka = hb + 160 + r16 * 2 * QP + g * 16, ka2 = hb + 160 + (r16 < 8 ? 32 + r16 : 39) * 2 * QP + g * 16;
    unsigned va = hb + 320 + (4 * g + q4) * 2 * QP + p4 * 8, va2 = hb + 320 + (g < 2 ? 32 + 4 * g + q4 : 39) * 2 * QP + p4 * 8;
    unsigned oa = OIM + (2 * r16 + par) * OP + hd * 40 + g * 8;
    f32x4 ba, bb, b0;
#pragma unroll
    for (int r = 0; r < 4; ++r) {
      const int d = r16 - 4 * g - r;
      ba[r] = (d + 8 >= 0 && d + 8 <= 7) ? 0.f : -1e30f;
      bb[r] = (d - 8 >= 0 && d - 8 <= 7) ? 0.f : -1e30f;
      b0[r] = (st == 0 && g < 2) ? -1e30f : ba[r];
    }
    unsigned m3 = g < 3 ? ~0u : 0u, m2 = g < 2 ? ~0u : 0u;
    PIN(ka); PIN(ka2); PIN(va); PIN(va2); PIN(oa); PIN(ba); PIN(bb); PIN(b0); PIN(m3); PIN(m2);
    BAR();
    STAMP(4);
    v4u kf[3], qf[2];
    v2u vt[3][2];
#pragma unroll
    for (int t = 0; t < 2; ++t) { const unsigned a = ka - 160 + (8 + t * 16) * 2 * QP; qf[t] = cat(lds64(a), lds64(a + 8)); }
    kf[0] = cat(lds64(ka), lds64(ka + 8));
    kf[1] = cat(lds64(ka + 32 * QP), lds64(ka + 32 * QP + 8));
    kf[2] = cat(lds64(ka2), lds64(ka2 + 8));
    vt[0][0] = ldstr(va); vt[1][0] = ldstr(va + 32 * QP); vt[2][0] = ldstr(va2);
    vt[0][1] = ldstr(va + 32); vt[1][1] = ldstr(va + 32 * QP + 32); vt[2][1] = ldstr(va2 + 32);
    SCHED();
    f32x4 sa[2], sb_[2];
#pragma unroll
    for (int t = 0; t < 2; ++t) {
      const v4u q = v4u{qf[t].x & m3, qf[t].y & m3, qf[t].z & m2, qf[t].w & m2};
      sa[t] = mfma(kf[t], q, t ? ba : b0); sb_[t] = mfma(kf[t + 1], q, bb);
    }
    constexpr float C = 0.22360679774997896f * 1.4426950408889634f;
    float mc[2];
#pragma unroll
    for (int t = 0; t < 2; ++t)
      mc[t] = mx2(mx2(mx2(sa[t].x, sa[t].y), mx2(sa[t].z, sa[t].w)), mx2(mx2(sb_[t].x, sb_[t].y), mx2(sb_[t].z, sb_[t].w)));
#pragma unroll
    for (int t = 0; t < 2; ++t) mc[t] = rows_max(mc[t]) * C;
    f32x4 o0[2], o1[2], sum[2];
#pragma unroll
    for (int t = 0; t < 2; ++t) {
      float p[8];
#pragma unroll
      for (int r = 0; r < 4; ++r) { p[r] = __builtin_amdgcn_exp2f(fmaf(sa[t][r], C, -mc[t])); p[4 + r] = __builtin_amdgcn_exp2f(fmaf(sb_[t][r], C, -mc[t])); }
      const v4u pf = v4u{pk(p[0], p[1]), pk(p[2], p[3]), pk(p[4], p[5]), pk(p[6], p[7])};
      const f32x4 z = f32x4{0.f, 0.f, 0.f, 0.f};
      sum[t] = mfma(v4u{ONES, ONES, ONES, ONES}, pf, z);
      o0[t] = mfma(cat(vt[t][0], vt[t + 1][0]), pf, z);
      o1[t] = mfma(cat(vt[t][1], vt[t + 1][1]), pf, z);
    }
    v2u ob1[2];
#pragma unroll
    for (int t = 0; t < 2; ++t) {
      const float inv = __builtin_amdgcn_rcpf(sum[t].x);
      sts64(oa + 32 * t * OP, v2u{pk(o0[t].x * inv, o0[t].y * inv), pk(o0[t].z * inv, o0[t].w * inv)});
      ob1[t] = v2u{pk(o1[t].x * inv, o1[t].y * inv), pk(o1[t].z * inv, o1[t].w * inv)};
    }
    if (g == 0) { sts64(oa + 32, ob1[0]); sts64(oa + 32 * OP + 32, ob1[1]); }
  }
  BAR();
  STAMP(5);
  const int tau = 8 * (r16 & 7) + 4 * (r16 >> 3);
  {
#pragma unroll
    for (int pass = 0; pass < 2; ++pass) {
      if (pass == 0 || w < 2) {
        const int ci = pass * 8 + w, c8 = dt * 10 + ci;
        const v4u v = lds128(OIM + (tau + g) * OP + ci * 16);
        __builtin_amdgcn_raw_buffer_store_b128(v, rs, OF_OFF + ((st * 4 + g) * 20 + (c8 >> 2)) * 1024 + ((c8 & 3) * 16 + r16) * 16, 0, 16);
      }
    }
  }
  WAITV(0);
  PIN(ep);
  const unsigned tag = __builtin_amdgcn_readfirstlane(ep) + 1u;
  BAR();
  if (tid == 0) __builtin_amdgcn_raw_buffer_store_b32(tag, rs, F1_OFF + st * 128 + dt * 4, 0, 16);
  STAMP(6);

  const int ej = lane >> 5, ehalf = (lane >> 4) & 1;
  const int etok = tau + 2 * (w & 1) + ej;
  v4u hres[2];
  {
    const unsigned xo = (s0 + etok) * 1280 + dt * 160 + ehalf * 16;
    gld16(hres[0], xo + (w >> 1) * 32, x);
    gld16(hres[1], xo + 128, x);
  }
#pragma unroll
  for (int n = N1Z; n < N1A; ++n) ldw1(n);
  {
    const int kh = w >> 2, tj = w & 3;
    wait_flags(ws, F1_OFF + st * 128 + (kh * 4 + (lane & 3)) * 4, tag);
    STAMP(7);
    cptr ob = ws + OF_OFF + (size_t)((st * 4 + tj) * 20 + kh * 10) * 1024;
    v4u of[10];
#pragma unroll
    for (int j = 0; j < 10; ++j) {
      int kq = j + rot2;
      kq = kq >= 10 ? kq - 10 : kq;
      gld16c<0>(of[j], lo, ob + kq * 1024);
    }
    f32x4 acc[5];
#pragma unroll
    for (int c = 0; c < 5; ++c) acc[c] = f32x4{0.f, 0.f, 0.f, 0.f};
    const unsigned wl = trl + kh * 5120;
    v4u af[2][5];
#pragma unroll
    for (int c = 0; c < 5; ++c) { const unsigned a = 81920 + wl + c * 32; af[0][c] = cat(ldstr(a), ldstr(a + 1280)); }
#pragma unroll
    for (int j = 0; j < 10; ++j) {
      if (j < 9) {
#pragma unroll
        for (int c = 0; c < 5; ++c) {
          const unsigned a = (j + 1 < 8 ? 81920 + (j + 1) * 10240 : (j + 1 - 8) * 10240) + wl + c * 32;
          af[(j + 1) & 1][c] = cat(ldstr(a), ldstr(a + 1280));
        }
      }
      WAITV(9 - j + (j < N1B ? j : N1B));
      PIN(of[j]);
#pragma unroll
      for (int c = 0; c < 5; ++c) acc[c] = mfma(af[j & 1][c], of[j], acc[c]);
      SCHED();
      if (j < N1B) ldw1(N1A + j);
    }
    STAMP(8);
    constexpr unsigned RED = 40960, HIM = 21504, PST = 32768, GST = 40960;
#pragma unroll
    for (int c = 0; c < 5; ++c) sts128f(RED + ((kh * 5 + c) * 4 + tj) * 1024 + lo, acc[c]);
    BAR();
    PIN(hres[0]); PIN(hres[1]);
#pragma unroll
    for (int pass = 0; pass < 2; ++pass) {
      if (pass == 0 || w < 2) {
        const int P = pass * 8 + w, ct = P >> 1;
        const unsigned ra = RED + (ct * 4 + 2 * (w & 1) + ej) * 1024 + (ehalf * 32 + r16) * 16;
        const f32x4 a0 = lds128f(ra) + lds128f(ra + 20480), a1 = lds128f(ra + 256) + lds128f(ra + 256 + 20480);
        const v4u xv = hres[pass];
        v4u h;
        h.x = pk(a0.x + blo(xv.x), a0.y + bhi(xv.x)); h.y = pk(a0.z + blo(xv.y), a0.w + bhi(xv.y));
        h.z = pk(a1.x + blo(xv.z), a1.y + bhi(xv.z)); h.w = pk(a1.z + blo(xv.w), a1.w + bhi(xv.w));
        hres[pass] = h;
        float sm = 0.f, sq = 0.f;
#pragma unroll
        for (int e = 0; e < 4; ++e) { sm = dot2(h[e], ONES, sm); sq = dot2(h[e], h[e], sq); }
        sts64f(PST + ((ct * 2 + ehalf) * 64 + etok) * 8, f32x2{sm, sq});
      }
    }
    BAR();
    if (w == 0) {
      f32x2 s = f32x2{0.f, 0.f};
#pragma unroll
      for (int i = 0; i < 10; ++i) s += lds64f(PST + (i * 64 + lane) * 8);
      __builtin_amdgcn_raw_buffer_store_b128(v4u{asu(s.x), tag, asu(s.y), tag}, rs, GR_OFF + (st * 8 + dt) * 1024 + lo, 0, 16);
    }
    STAMP(22);
    {
      v4u gv;
      const unsigned go = GR_OFF + (st * 8 + w) * 1024 + lo;
#pragma unroll 1
      for (unsigned spin = 0; spin < SPIN_CAP; ++spin) {
        asm volatile("global_load_dwordx4 %0, %1, %2 sc1\n\ts_waitcnt vmcnt(0)" : "=v"(gv) : "v"(go), "s"(ws) : "memory");
        if (__builtin_amdgcn_ballot_w64(gv.y != tag || gv.w != tag) == 0ull) break;
        __builtin_amdgcn_s_sleep(1);
      }
      sts64(GST + (w * 64 + lane) * 8, v2u{gv.x, gv.z});
    }
    STAMP(23);
    BAR();
    {
      f32x2 s = f32x2{0.f, 0.f};
#pragma unroll
      for (int i = 0; i < 8; ++i) s += lds64f(GST + (i * 64 + etok) * 8);
      const float mean = s.x * (1.f / D_), var = fmaxf(s.y * (1.f / D_) - mean * mean, 0.f);
      const float r1 = __builtin_amdgcn_rsqf(var + EPS), r2 = __builtin_amdgcn_rsqf(var * r1 * r1 + EPS);
      const float a = r1 * r2, c = -mean * a;
#pragma unroll
      for (int pass = 0; pass < 2; ++pass) {
        if (pass == 0 || w < 2) {
          const int ct = (pass * 8 + w) >> 1;
          const v4u h = hres[pass];
          v4u hn;
#pragma unroll
          for (int e = 0; e < 4; ++e) hn[e] = pk(fmaf(blo(h[e]), a, c), fmaf(bhi(h[e]), a, c));
          sts128(HIM + swap23(etok) * 160 + ct * 32 + ehalf * 16, hn);
        }
      }
    }
    BAR();
#pragma unroll
    for (int pass = 0; pass < 2; ++pass) {
      if (pass == 0 || w < 2) {
        const int P = pass * 8 + w, ct = P >> 1, ks = P & 1;
        const unsigned a = HIM + ks * 5120 + trl + ct * 32;
        const v4u v = cat(ldstr(a), ldstr(a + 1280));
        __builtin_amdgcn_raw_buffer_store_b128(v, rs, HF_OFF + ((dt * 5 + ct) * 64 + st * 2 + ks) * 1024 + lo, 0, 16);
      }
    }
    STAMP(9);
    WAITV(0);
    BAR();
    if (tid == 0) __builtin_amdgcn_raw_buffer_store_b32(tag, rs, F2_OFF + (st * 8 + dt) * 4, 0, 16);
    STAMP(10);
  }

  v4u vr[6][4];
  const int rot4 = (idx & 7) % 6, rot5 = (idx >> 3);
  unsigned w2v;
#define RELANE() do { lane = threadIdx.x & 63; PIN(lane); g = lane >> 4; r16 = lane & 15; lo = lane * 16; } while (0)
#define MKW2V() w2v = (32 * (g & 1) + 4 * (g >> 1) + (r16 >> 3)) * 4096u + (r16 & 7) * 16
  cptr w2b = w2 + (size_t)w * (192 * 4096) + st * 128;
  auto ldw2 = [&](int n) {
    int kk = (n >> 2) + rot4;
    kk = kk >= 6 ? kk - 6 : kk;
    gld16(vr[n >> 2][n & 3], w2v, w2b + (size_t)((kk >> 1) * 64 + (kk & 1) * 2 + (n & 3) * 8) * 4096);
  };
  if (p3) {
    unsigned bias[4];
    {
      const int fb = f3 * 64 + 32 * (g & 1) + 4 * (g >> 1) + 2 * (w >> 2);
#pragma unroll
      for (int r = 0; r < 4; ++r) gld4(bias[r], (fb + 8 * r) * 2, b1);
    }
#pragma unroll
    for (int n = N1A + N1B; n < 32; ++n) ldw1(n);
#pragma unroll
    for (int kk = 0; kk < 8; ++kk) {
      if (4 * kk + 4 > N1A + N1B) WAITV(32 - (4 * kk + 4));
#pragma unroll
      for (int j = 0; j < 4; ++j) PIN(wr[kk][j]);
      xpose(wr[kk]);
#pragma unroll
      for (int j = 0; j < 4; ++j) PIN(wr[kk][j]);
    }
    STAMP(11);
    wait_flags(ws, F2_OFF + ((w * 4 + (lane & 3)) * 8 + (d3 * 64 + ((lane >> 2) & 1) * 63) / 80) * 4, tag);
    STAMP(12);
    RELANE();
    constexpr int HR = 12;
    v4u hf[HR];
    cptr hb = ws + HF_OFF + (size_t)(d3 * 4 * 64 + w * 8) * 1024;
    auto ldh = [&](int n) {
      gld16c<0>(hf[n % HR], lo, hb + (size_t)(((n >> 3) + rot3d) & 3) * 65536 + (((n & 7) + rot3) & 7) * 1024);
    };
#pragma unroll
    for (int n = 0; n < HR; ++n) ldh(n);
    STAMP(13);
#pragma unroll
    for (int dj = 0; dj < 4; ++dj) {
      f32x4 acc[4];
#pragma unroll
      for (int i = 0; i < 4; ++i) acc[i] = f32x4{0.f, 0.f, 0.f, 0.f};
#pragma unroll
      for (int kk = 0; kk < 8; ++kk) {
        const int n = dj * 8 + kk;
        WAITV(31 - n < HR - 1 ? 31 - n : HR - 1);
        PIN(hf[n % HR]);
#pragma unroll
        for (int i = 0; i < 4; ++i) acc[i] = mfma(wr[kk][i], hf[n % HR], acc[i]);
        SCHED();
        if (n + HR < 32) ldh(n + HR);
      }
#pragma unroll
      for (int i = 0; i < 4; ++i) sts128f(w * 16384 + (i * 4 + ((dj + rot3d) & 3)) * 1024 + lo, acc[i]);
    }
    STAMP(14);
    RELANE(); MKW2V();
#pragma unroll
    for (int n = 0; n < N2A; ++n) ldw2(n);
    BAR();
    if (b == 0 && tid == 0) __builtin_amdgcn_raw_buffer_store_b32(tag, rs, EP_OFF, 0, 16);
    {
      const int dj = w & 3, kp = w >> 2;
      const unsigned ra = (2 * kp * 4 + dj) * 1024 + lo;
      f32x4 s0_ = f32x4{0.f, 0.f, 0.f, 0.f}, s1_ = s0_;
#pragma unroll
      for (int v = 0; v < 8; ++v) { s0_ += lds128f(ra + v * 16384); s1_ += lds128f(ra + v * 16384 + 4096); }
#pragma unroll
      for (int r = 0; r < 4; ++r) PIN(bias[r]);
      v4u u;
      u.x = pk(gelu_tanh(s0_.x + blo(bias[0])), gelu_tanh(s0_.y + blo(bias[1])));
      u.y = pk(gelu_tanh(s0_.z + blo(bias[2])), gelu_tanh(s0_.w + blo(bias[3])));
      u.z = pk(gelu_tanh(s1_.x + bhi(bias[0])), gelu_tanh(s1_.y + bhi(bias[1])));
      u.w = pk(gelu_tanh(s1_.z + bhi(bias[2])), gelu_tanh(s1_.w + bhi(bias[3])));
      __builtin_amdgcn_raw_buffer_store_b128(u, rs, UF_OFF + ((d3 * 4 + dj) * 48 + f3 * 2 + kp) * 1024 + lo, 0, 16);
    }
    STAMP(15);
    WAITV(0);
    BAR();
    if (tid == 0) __builtin_amdgcn_raw_buffer_store_b32(tag, rs, F3_OFF + d3 * 128 + f3 * 4, 0, 16);
    STAMP(16);
  } else {
    RELANE(); MKW2V();
#pragma unroll
    for (int n = 0; n < N2A; ++n) ldw2(n);
    WAITV(0);
  }

  {
#pragma unroll
    for (int n = N2A; n < 24; ++n) ldw2(n);
    const int ej = lane >> 5, ehalf = (lane >> 4) & 1;
    const int etok = 8 * (r16 & 7) + 4 * (r16 >> 3) + 2 * (w & 1) + ej;
    unsigned bq;
    asm volatile("global_load_ushort %0, %1, %2" : "=v"(bq) : "v"((s0 + etok) * 2), "s"(b2) : "memory");
#pragma unroll
    for (int kk = 0; kk < 6; ++kk) {
      WAITV(1 + 24 - (4 * kk + 4));
#pragma unroll
      for (int j = 0; j < 4; ++j) PIN(vr[kk][j]);
      xpose(vr[kk]);
#pragma unroll
      for (int j = 0; j < 4; ++j) PIN(vr[kk][j]);
    }
    STAMP(17);
    {
      const int l6 = lane % 6;
      wait_flags(ws, F3_OFF + ((dt * 80) / 64 + (l6 & 1)) * 128 + (3 * w + (l6 >> 1)) * 4, tag);
    }
    STAMP(18);
    constexpr int UR = 18;
    v4u uf[UR];
    cptr ub = ws + UF_OFF + (size_t)(dt * 5 * 48 + w * 6) * 1024;
    auto ldu = [&](int n) {
      int c = n / 6 + rot5, kk = n % 6 + rot4;
      c = c >= 5 ? c - 5 : c;
      kk = kk >= 6 ? kk - 6 : kk;
      gld16c<0>(uf[n % UR], lo, ub + (size_t)c * 49152 + kk * 1024);
    };
#pragma unroll
    for (int n = 0; n < UR; ++n) ldu(n);
#pragma unroll
    for (int c = 0; c < 5; ++c) {
      f32x4 acc[4];
#pragma unroll
      for (int j = 0; j < 4; ++j) acc[j] = f32x4{0.f, 0.f, 0.f, 0.f};
#pragma unroll
      for (int kk = 0; kk < 6; ++kk) {
        const int n = c * 6 + kk;
        WAITV(29 - n < UR - 1 ? 29 - n : UR - 1);
        PIN(uf[n % UR]);
#pragma unroll
        for (int j = 0; j < 4; ++j) acc[j] = mfma(uf[n % UR], vr[kk][j], acc[j]);
        SCHED();
        if (n + UR < 30) ldu(n + UR);
      }
      int cr = c + rot5;
      cr = cr >= 5 ? cr - 5 : cr;
#pragma unroll
      for (int j = 0; j < 4; ++j) sts128f(w * 20480 + (cr * 4 + j) * 1024 + lo, acc[j]);
    }
    STAMP(19);
    BAR();
    PIN(bq);
    const float bias = blo(bq);
#pragma unroll
    for (int pass = 0; pass < 2; ++pass) {
      if (pass == 0 || w < 2) {
        const int P = pass * 8 + w, ct = P >> 1;
        const unsigned ra = (ct * 4 + 2 * (w & 1) + ej) * 1024 + (ehalf * 32 + r16) * 16;
        f32x4 s0_ = f32x4{0.f, 0.f, 0.f, 0.f}, s1_ = s0_;
#pragma unroll
        for (int v = 0; v < 8; ++v) { s0_ += lds128f(ra + v * 20480); s1_ += lds128f(ra + v * 20480 + 256); }
        const v4u hv = hres[pass];
        v4u o;
        o.x = pk(s0_.x + bias + blo(hv.x), s0_.y + bias + bhi(hv.x)); o.y = pk(s0_.z + bias + blo(hv.y), s0_.w + bias + bhi(hv.y));
        o.z = pk(s1_.x + bias + blo(hv.z), s1_.y + bias + bhi(hv.z)); o.w = pk(s1_.z + bias + blo(hv.w), s1_.w + bias + bhi(hv.w));
        *(GAS v4u*)(out + (size_t)(s0 + etok) * 1280 + dt * 160 + ct * 32 + ehalf * 16) = o;
      }
    }
    STAMP(20);
  }
  asm volatile(".Lfb_end:");
}

extern "C" void kernel_launch(void* const* d_in, const int* in_sizes, int n_in, void* d_out, int out_size, void* d_ws,
                              size_t ws_size, hipStream_t stream) {
  (void)hipFuncSetAttribute((const void*)fused_block, hipFuncAttributeMaxDynamicSharedMemorySize, 163840);
  fused_block<<<256, 512, 163840, stream>>>((cptr)d_in[0], (cptr)d_in[1], (cptr)d_in[2], (cptr)d_in[4], (cptr)d_in[5], (cptr)d_in[6],
                                            (cptr)d_in[7], (unsigned char*)d_out, (unsigned char*)d_ws);
}
```

```cpp
#include <hip/hip_runtime.h>
#include <hip/hip_bf16.h>

typedef unsigned v4u __attribute__((ext_vector_type(4)));
typedef unsigned v2u __attribute__((ext_vector_type(2)));
typedef float f32x4 __attribute__((ext_vector_type(4)));
typedef float f32x2 __attribute__((ext_vector_type(2)));
typedef short v4s __attribute__((ext_vector_type(4)));
typedef __bf16 bf16x8 __attribute__((ext_vector_type(8)));
typedef __bf16 bf16x2 __attribute__((ext_vector_type(2)));
#define LAS __attribute__((address_space(3)))
#define GAS __attribute__((address_space(1)))
typedef const unsigned char* cptr;

constexpr int D_ = 640;
constexpr unsigned OF_OFF = 0;
constexpr unsigned HF_OFF = OF_OFF + 2621440;
constexpr unsigned UF_OFF = HF_OFF + 2621440;
constexpr unsigned GR_OFF = UF_OFF + 1966080;
constexpr unsigned F1_OFF = GR_OFF + 262144;
constexpr unsigned F2_OFF = F1_OFF + 8192;
constexpr unsigned F3_OFF = F2_OFF + 16384;
constexpr unsigned EP_OFF = F3_OFF + 10240;
constexpr unsigned WS_BYTES = EP_OFF + 256;
constexpr float EPS = 1e-5f;
constexpr unsigned SPIN_CAP = 1u << 16;

#define WAITV(n) asm volatile("s_waitcnt vmcnt(%0)" ::"i"(n) : "memory")
#define BAR() asm volatile("s_waitcnt lgkmcnt(0)\n\ts_barrier" ::: "memory")
#define PIN(x) asm volatile("" : "+v"(x))
#define SCHED() __builtin_amdgcn_sched_barrier(0)

__device__ __forceinline__ void dma16(unsigned voff, cptr base, unsigned ldsaddr) {
  asm volatile("s_mov_b32 m0, %2\n\ts_nop 0\n\tglobal_load_lds_dwordx4 %0, %1" ::"v"(voff), "s"(base), "s"(ldsaddr) : "memory");
}
__device__ __forceinline__ void dma16m(unsigned voff, cptr base, unsigned ldsaddr, unsigned long long mask) {
  asm volatile("s_mov_b32 m0, %2\n\ts_mov_b64 exec, %3\n\tglobal_load_lds_dwordx4 %0, %1\n\ts_mov_b64 exec, -1" ::"v"(voff), "s"(base), "s"(ldsaddr), "s"(mask) : "memory");
}
__device__ __forceinline__ void gld16(v4u& dst, unsigned voff, cptr base) {
  asm volatile("global_load_dwordx4 %0, %1, %2" : "=v"(dst) : "v"(voff), "s"(base) : "memory");
}
template <int OFF> __device__ __forceinline__ void gld16c(v4u& dst, unsigned voff, cptr base) {
  asm volatile("global_load_dwordx4 %0, %1, %2 offset:%3 sc1" : "=v"(dst) : "v"(voff), "s"(base), "i"(OFF) : "memory");
}
__device__ __forceinline__ void gld4(unsigned& dst, unsigned voff, cptr base) {
  asm volatile("global_load_dword %0, %1, %2" : "=v"(dst) : "v"(voff), "s"(base) : "memory");
}

__device__ __forceinline__ v4u lds128(unsigned a) { return *(const LAS v4u*)(size_t)a; }
__device__ __forceinline__ f32x4 lds128f(unsigned a) { return *(const LAS f32x4*)(size_t)a; }
__device__ __forceinline__ v2u lds64(unsigned a) { return *(const LAS v2u*)(size_t)a; }
__device__ __forceinline__ f32x2 lds64f(unsigned a) { return *(const LAS f32x2*)(size_t)a; }
__device__ __forceinline__ float lds32f(unsigned a) { return *(const LAS float*)(size_t)a; }
__device__ __forceinline__ void sts128(unsigned a, v4u v) { *(LAS v4u*)(size_t)a = v; }
__device__ __forceinline__ void sts128f(unsigned a, f32x4 v) { *(LAS f32x4*)(size_t)a = v; }
__device__ __forceinline__ void sts64(unsigned a, v2u v) { *(LAS v2u*)(size_t)a = v; }
__device__ __forceinline__ void sts64f(unsigned a, f32x2 v) { *(LAS f32x2*)(size_t)a = v; }
__device__ __forceinline__ void sts32f(unsigned a, float v) { *(LAS float*)(size_t)a = v; }
__device__ __forceinline__ v2u ldstr(unsigned a) {
  return __builtin_bit_cast(v2u, __builtin_amdgcn_ds_read_tr16_b64_v4i16((LAS v4s*)(size_t)a));
}
__device__ __forceinline__ v4u cat(v2u a, v2u b) { return v4u{a.x, a.y, b.x, b.y}; }
__device__ __forceinline__ f32x4 mfma(v4u a, v4u b, f32x4 c) {
  return __builtin_amdgcn_mfma_f32_16x16x32_bf16(__builtin_bit_cast(bf16x8, a), __builtin_bit_cast(bf16x8, b), c, 0, 0, 0);
}
__device__ __forceinline__ float dot2(unsigned a, unsigned b, float c) {
  return __builtin_amdgcn_fdot2_f32_bf16(__builtin_bit_cast(bf16x2, a), __builtin_bit_cast(bf16x2, b), c, false);
}
__device__ __forceinline__ unsigned pk(float lo, float hi) {
  return __builtin_bit_cast(unsigned, __builtin_convertvector(f32x2{lo, hi}, bf16x2));
}
__device__ __forceinline__ float blo(unsigned u) { return __builtin_bit_cast(float, u << 16); }
__device__ __forceinline__ float bhi(unsigned u) { return __builtin_bit_cast(float, u & 0xffff0000u); }
template <int CTRL> __device__ __forceinline__ float dpp(float v) {
  return __builtin_bit_cast(float, __builtin_amdgcn_mov_dpp(__builtin_bit_cast(int, v), CTRL, 0xF, 0xF, true));
}
__device__ __forceinline__ float asf(unsigned u) { return __builtin_bit_cast(float, u); }
__device__ __forceinline__ unsigned asu(float f) { return __builtin_bit_cast(unsigned, f); }
__device__ __forceinline__ float mx2(float a, float b) { return __builtin_elementwise_maximum(a, b); }
__device__ __forceinline__ float rows_max(float v) {
  const v2u a = __builtin_amdgcn_permlane16_swap(asu(v), asu(v), false, false);
  v = mx2(asf(a.x), asf(a.y));
  const v2u c = __builtin_amdgcn_permlane32_swap(asu(v), asu(v), false, false);
  return mx2(asf(c.x), asf(c.y));
}
__device__ __forceinline__ float rows_sum(float v) {
  const v2u a = __builtin_amdgcn_permlane16_swap(asu(v), asu(v), false, false);
  v = asf(a.x) + asf(a.y);
  const v2u c = __builtin_amdgcn_permlane32_swap(asu(v), asu(v), false, false);
  return asf(c.x) + asf(c.y);
}
__device__ __forceinline__ constexpr unsigned swap23(unsigned r) { return (r & ~12u) | ((r & 4u) << 1) | ((r & 8u) >> 1); }
constexpr unsigned ONES = 0x3f803f80u;

__device__ __forceinline__ void xpose(v4u (&r)[4]) {
  unsigned lo[4][2], hi[4][2];
#pragma unroll
  for (int j = 0; j < 4; ++j)
#pragma unroll
    for (int q = 0; q < 2; ++q) {
      lo[j][q] = __builtin_amdgcn_update_dpp(r[j][q], r[j][2 + q], 0x128, 0xF, 0xC, false);
      hi[j][q] = __builtin_amdgcn_update_dpp(r[j][2 + q], r[j][q], 0x128, 0xF, 0x3, false);
    }
#pragma unroll
  for (int i = 0; i < 4; ++i) {
    const unsigned sel = (i & 1) ? 0x07060302u : 0x05040100u;
    r[i] = v4u{__builtin_amdgcn_perm(lo[1][i >> 1], lo[0][i >> 1], sel), __builtin_amdgcn_perm(lo[3][i >> 1], lo[2][i >> 1], sel),
               __builtin_amdgcn_perm(hi[1][i >> 1], hi[0][i >> 1], sel), __builtin_amdgcn_perm(hi[3][i >> 1], hi[2][i >> 1], sel)};
  }
}

__device__ __forceinline__ void wait_flags(cptr ws, unsigned voff, unsigned tag) {
#pragma unroll 1
  for (unsigned spin = 0; spin < SPIN_CAP; ++spin) {
    unsigned v;
    asm volatile("global_load_dword %0, %1, %2 sc1\n\ts_waitcnt vmcnt(0)" : "=v"(v) : "v"(voff), "s"(ws) : "memory");
    if (__builtin_amdgcn_ballot_w64(v != tag) == 0ull) break;
    __builtin_amdgcn_s_sleep(2);
  }
}
__device__ __forceinline__ float gelu_tanh(float v) {
  const float z = v * (-2.3022081986f - 0.10294324f * v * v);
  return v * __builtin_amdgcn_rcpf(1.f + __builtin_amdgcn_exp2f(z));
}

constexpr int STAGE = 40960;
constexpr int QP = 488, OP = 176;
constexpr int N1A = 12;
constexpr int N1B = 8;
constexpr int N2A = 8;
constexpr int SPOS = 3;
constexpr int n1b(int j) { return (j * N1B) / 10; }
constexpr int X1 = 5, X2 = 4;
constexpr int w2wait(int kk) { return 1 + 24 - (4 * kk + 4); }

__global__ __launch_bounds__(512, 2) void fused_block(cptr x, cptr wqkv, cptr wo, cptr w1, cptr b1, cptr w2, cptr b2,
                                                      unsigned char* out, unsigned char* wsp) {
  extern __shared__ __attribute__((aligned(16))) unsigned char smem[];
  unsigned long long pc; unsigned len;
  asm volatile("s_getpc_b64 %0\n.Lfb_here:\n\ts_mov_b32 %1, .Lfb_end-.Lfb_here\n\ts_max_i32 %1, %1, .Lfb_cend-.Lfb_here" : "=s"(pc), "=s"(len) :: "scc");
  unsigned junk;
  {
    unsigned off = ((blockIdx.x >> 3) * 16 + threadIdx.x) * 128;
    off = threadIdx.x < 16 && off < len ? off : 0;
    asm volatile("global_load_dword %0, %1, %2" : "=v"(junk) : "v"(off), "s"(pc) : "memory");
  }
  SCHED();
  cptr ws = wsp;
  const __amdgpu_buffer_rsrc_t rs = __builtin_amdgcn_make_buffer_rsrc((void*)wsp, 0, (int)WS_BYTES, 0x00020000);
  const int tid = threadIdx.x, w = __builtin_amdgcn_readfirstlane(tid >> 6);
  int lane = tid & 63;
  const int b = blockIdx.x, xcd = b & 7, idx = b >> 3;
  const int st = (xcd >> 1) * 8 + (idx & 7), dt = (xcd & 1) * 4 + (idx >> 3);
  const int s0 = st * 64;
  int g = lane >> 4, r16 = lane & 15, q4 = (lane & 15) >> 2, p4 = lane & 3;
  unsigned lo = lane * 16;
#define RELANE() do { lane = threadIdx.x & 63; PIN(lane); g = lane >> 4; r16 = lane & 15; q4 = r16 >> 2; p4 = lane & 3; lo = lane * 16; } while (0)

  const int pw = w & 3;
  const int rot2 = ((((idx & 7) * 5) >> 2) + 3 * (idx >> 3)) % 10;
  const int rot1 = rot2;
  constexpr unsigned STAT = 122880, QKV = 40960;
  const bool p3 = idx < 30;
  const int i3 = p3 ? idx : idx - 2;
  const int d3 = (xcd & 1) * 5 + i3 % 5, f3 = (xcd >> 1) * 6 + i3 / 5;
  const int rot3 = ((i3 % 5) * 8) / 5 + (xcd & 1);
  v4u wr[8][4];
  unsigned w1v[4];
  cptr w1b;
  auto ldw1 = [&](int n) {
    const int kk = ((n >> 2) + rot3) & 7;
    gld16(wr[n >> 2][n & 3], w1v[n & 3], w1b + kk * (32 * 3072));
  };
  auto ldw1r = [&](int n) {
    const int kk = ((n >> 2) + rot3) & 7;
    asm volatile("global_load_dwordx4 %0, %1, %2" : "+v"(wr[n >> 2][n & 3]) : "v"(w1v[n & 3]), "s"(w1b + kk * (32 * 3072)) : "memory");
  };
  auto xp1 = [&](int kk) {
#pragma unroll
    for (int j = 0; j < 4; ++j) PIN(wr[kk][j]);
    xpose(wr[kk]);
#pragma unroll
    for (int j = 0; j < 4; ++j) PIN(wr[kk][j]);
  };
  v2u xr[5];
  unsigned ep;
  const int at = w < 4 ? 1 : 0, ahp = w & 1, apar = (w >> 1) & 1;
  unsigned ka, ka2, va, va2, m3, m2;
  f32x4 ba, bb;
  auto consts = [&]() {
    const unsigned hb = QKV + (32 * at + apar) * QP + ahp * 80;
    ka = hb + 160 + r16 * 2 * QP + g * 16; ka2 = hb + 160 + (r16 < 8 ? 16 + r16 : 23) * 2 * QP + g * 16;
    va = hb + 320 + (4 * g + q4) * 2 * QP + p4 * 8; va2 = hb + 320 + (g < 2 ? 16 + 4 * g + q4 : 23) * 2 * QP + p4 * 8;
#pragma unroll
    for (int r = 0; r < 4; ++r) {
      const int d = r16 - 4 * g - r;
      ba[r] = (d + 8 >= 0 && d + 8 <= 7 && !(st == 0 && at == 0 && g < 2)) ? 0.f : -1e30f;
      bb[r] = (d - 8 >= 0 && d - 8 <= 7) ? 0.f : -1e30f;
    }
    m3 = g < 3 ? ~0u : 0u; m2 = g < 2 ? ~0u : 0u;
#pragma unroll
    for (int j = 0; j < 4; ++j) { w1v[j] = (16 * (g & 1) + 8 * (r16 >> 3) + (g >> 1) + 2 * j) * 3072u + (r16 & 7) * 16; PIN(w1v[j]); }
    w1b = w1;
    asm volatile("" : "+s"(w1b));
    w1b += (size_t)w * (256 * 3072) + f3 * 128;
    PIN(ka); PIN(ka2); PIN(va); PIN(va2); PIN(ba); PIN(bb); PIN(m3); PIN(m2);
  };
  unsigned wov[5];
  auto issue_wo = [&](int m) {
    const int q = pw + 4 * m;
    if (m < 4) {
#pragma unroll
      for (int i = 0; i < 5; ++i) dma16(wov[i], wo + ((q & 1) * 8 + (((q >> 1) + rot2) & 7)) * (40 * 1280), 81920 + q * 5120 + i * 1024);
    } else {
#pragma unroll
      for (int i = 0; i < 5; ++i) {
        const unsigned c = i * 64 + lane, G = swap23((c * 6554u) >> 16) >> 3;
        dma16(wov[i] + (36 * G + 16) * 1280u, wo + ((q & 1) * 8 + ((q >> 1) & 1) * 4) * (40 * 1280), pw * 5120 + i * 1024);
      }
    }
  };
  if (__builtin_expect(w >= 4, 1)) {
    unsigned xv[3], wv;
    const unsigned long long xmask = pw == 0 ? ~0xFFull : ~0ull;
    auto mkx = [&](int i) {
      const int row = 20 * pw + 8 * i + (lane >> 3), ch = (lane & 7) ^ ((row >> 1) & 7);
      int tok = s0 - 16 + row;
      tok = tok < 0 ? 0 : (tok > 2047 ? 2047 : tok);
      xv[i] = tok * 1280 + ch * 16;
    };
    auto rs_ = [&](int s) { int sr = s + rot1; return sr >= 10 ? sr - 10 : sr; };
    auto issx = [&](int s, int i) {
      cptr bx = x + rs_(s) * 128;
      const unsigned l = (s & 3) * STAGE + pw * 2560 + i * 1024;
      if (i == 0) dma16m(xv[0], bx, l, xmask);
      else if (i == 1) dma16(xv[1], bx, l);
      else dma16m(xv[2], bx, l, 0xFFFFFFFFull);
    };
    auto issw = [&](int s, int i0, int i1) {
      cptr bw = wqkv + (size_t)(rs_(s) * 64 + pw * 16) * 3840;
#pragma unroll
      for (int i = i0; i < i1; ++i) dma16m(wv, bw + swap23(2 * i) * 3840, (s & 3) * STAGE + 10240 + (pw * 8 + i) * 960, (1ull << 60) - 1);
    };
    auto issue = [&](int s, int part = 3) {
      if (part & 1) issw(s, 0, SPOS);
      if (part & 2) { issw(s, SPOS, 8); issx(s + 1, 0); issx(s + 1, 1); issx(s + 1, 2); }
    };
    mkx(0); mkx(1); mkx(2);
    {
      const int a = 4 - (dt & 3), bb_ = lane >= 30 ? 1 : 0, c = lane - 30 * bb_, p = c >> 1;
      const bool sec = p >= 3 * a;
      const int pp = sec ? p - 3 * a : p, n = sec ? 5 - a : a;
      const int seg = (pp >= n ? 1 : 0) + (pp >= 2 * n ? 1 : 0), t = pp - seg * n + (sec ? a : 0);
      wv = bb_ * 3840 + seg * 1280 + dt * 160 + t * 32 + (c & 1) * 16;
    }
    PIN(xv[0]); PIN(xv[1]); PIN(xv[2]); PIN(wv);
    SCHED();
    issx(0, 0); issx(0, 1); issx(0, 2);
    issue(0);
    asm volatile("global_load_dword %0, %1, %2 sc1" : "=v"(ep) : "v"(EP_OFF), "s"(ws) : "memory");
#pragma unroll
    for (int i = 0; i < 5; ++i) {
      const unsigned c = i * 64 + lane, R = (c * 6554u) >> 16, ch = c - R * 10u;
      const unsigned k = swap23(R);
      wov[i] = (20 * ((k >> 2) & 1) + 4 * (k >> 3) + (k & 3)) * 1280u + dt * 160 + ch * 16;
      PIN(wov[i]);
    }
    consts();
    const int xrow = 16 + 32 * (pw >> 1) + 2 * r16 + (pw & 1);
    unsigned xo = xrow * 128 + (((g >> 1) ^ ((xrow >> 1) & 7)) * 16) + (g & 1) * 8;
    PIN(xo);
#pragma unroll
    for (int c = 0; c < 5; ++c) xr[c] = v2u{0u, 0u};
    f32x4 gram[2] = {f32x4{0.f, 0.f, 0.f, 0.f}, f32x4{0.f, 0.f, 0.f, 0.f}};
    unsigned sa[2];
#pragma unroll
    for (int t = 0; t < 2; ++t) {
      const int srow = r16 < 10 ? 20 * pw + 10 * t + r16 : 0;
      sa[t] = srow * 128 + ((g ^ ((srow >> 1) & 7)) * 16);
      PIN(sa[t]);
    }
    auto stat = [&](int s) {
      const unsigned sb = (s & 3) * STAGE;
      const int rel = rs_(s) * 4 - dt * 5;
      if (rel > -4 && rel < 5) {
#pragma unroll
        for (int c = 0; c < 5; ++c) {
          const v2u v = lds64((sb + xo) ^ (((c - rel) & 3) * 32));
          const bool take = c - rel >= 0 && c - rel < 4;
          xr[c].x = take ? v.x : xr[c].x; xr[c].y = take ? v.y : xr[c].y;
        }
      }
      v4u sx[2][2];
#pragma unroll
      for (int t = 0; t < 2; ++t) { sx[t][0] = lds128(sb + sa[t]); sx[t][1] = lds128((sb + sa[t]) ^ 64u); }
#pragma unroll
      for (int t = 0; t < 2; ++t) { gram[t] = mfma(sx[t][0], sx[t][0], gram[t]); gram[t] = mfma(sx[t][1], sx[t][1], gram[t]); }
    };
    issue(1);
    WAITV(11);
    PIN(ep); PIN(junk);
    BAR();
    stat(0); stat(1);
    issue(2);
#pragma unroll 1
    for (int s = 0; s < 6; ++s) {
      WAITV(11); BAR();
      issue(s + 3, 1);
      stat(s + 2);
      issue(s + 3, 2);
    }
    WAITV(11); BAR();
    stat(8);
    issw(9, 0, 8);
    WAITV(8); BAR();
    stat(9);
#pragma unroll
    for (int t = 0; t < 2; ++t) {
      const float dg = p4 == 0 ? gram[t].x : p4 == 1 ? gram[t].y : p4 == 2 ? gram[t].z : gram[t].w;
      if (r16 < 10 && (g == q4 || g == 3)) sts32f(STAT + (20 * pw + 10 * t + r16) * 8 + (g == 3 ? 0 : 4), g == 3 ? gram[t].w : dg);
    }
    issue_wo(0);
    WAITV(5); BAR();
    issue_wo(1); issue_wo(3);
    BAR();
    issue_wo(4);
#pragma unroll
    for (int n = 0; n < N1A; ++n) ldw1(n);
  } else {
    asm volatile("global_load_dword %0, %1, %2 sc1" : "=v"(ep) : "v"(EP_OFF), "s"(ws) : "memory");
    unsigned junk1 = 0, junk2 = 0, junk3 = 0;
    {
      cptr p; unsigned lim, k;
      if (lane < 16) { p = ws; lim = WS_BYTES; k = lane; }
      else if (lane < 24) { p = w1; lim = 6291456; k = lane - 16; }
      else if (lane < 32) { p = w2; lim = 6291456; k = lane - 24; }
      else if (lane < 36) { p = out; lim = 2621440; k = lane - 32; }
      else if (lane < 38) { p = wo; lim = 819200; k = lane - 36; }
      else if (lane == 38) { p = b1; lim = 3072; k = 0; }
      else { p = b2; lim = 4096; k = 0; }
      unsigned off = k * 1048576u;
      off = off < lim ? off : lim - 4;
      if (w == 2) asm volatile("global_load_dword %0, %1, off sc1" : "+v"(junk1) : "v"(p + off) : "memory");
    }
    if (tid < 32) sts128((tid >> 3) * STAGE + (tid & 7) * 16, v4u{ONES, ONES, ONES, ONES});
    consts();
    const unsigned trl = 10240 + ((g >> 1) * 16 + (g & 1) * 4 + q4) * 480 + p4 * 8;
    unsigned wa[4];
#pragma unroll
    for (int i = 0; i < 4; ++i) {
      const int ct = pw + 4 * i < 15 ? pw + 4 * i : 14, a = 4 - (dt & 3), sg = ct / 5, t = ct % 5;
      wa[i] = trl + (t < a ? sg * a + t : 3 * a + sg * (5 - a) + t - a) * 32;
    }
    const unsigned xa0 = r16 * 128 + ((g ^ ((r16 >> 1) & 7)) * 16), xa1 = xa0 ^ 64u;
    f32x4 acc1[4][5];
#pragma unroll
    for (int i = 0; i < 4; ++i)
#pragma unroll
      for (int j = 0; j < 5; ++j) acc1[i][j] = f32x4{0.f, 0.f, 0.f, 0.f};
    v4u af[2][4], xb[2][5];
    auto half = [&](int kc, bool rd, unsigned sb) {
      const int kn = kc ^ 1;
      const unsigned xa = sb + (kn ? xa1 : xa0);
      v2u t0;
#pragma unroll
      for (int n = 0; n < 20; ++n) {
        const int j = 4 - n / 4, i = n & 3;
        if (rd) {
          if (n == 0) xb[kn][4] = lds128(xa + 8192);
          if (n >= 1 && n <= 8) {
            const int a = (n - 1) >> 1;
            if (n & 1) t0 = ldstr(sb + wa[a] + kn * 15360);
            else af[kn][a] = cat(t0, ldstr(sb + wa[a] + kn * 15360 + 3840));
          }
          if (n >= 9 && n <= 12) xb[kn][12 - n] = lds128(xa + (12 - n) * 2048);
        }
        acc1[i][j] = mfma(af[kc][i], xb[kc][j], acc1[i][j]);
        SCHED();
      }
    };
    BAR();
#pragma unroll
    for (int j = 4; j >= 0; --j) xb[0][j] = lds128(xa0 + j * 2048);
#pragma unroll
    for (int i = 0; i < 4; ++i) af[0][i] = cat(ldstr(wa[i]), ldstr(wa[i] + 3840));
    const int cu64 = (xcd & 1) * 32 + idx, ts = 1 + ((idx + (xcd >> 1)) & 3);
    const unsigned t2o = (cu64 * 24 + pw * 6 + (lane < 48 ? lane >> 3 : 0)) * 4096u + (lane & 7) * 128;
    const int ts1 = ts + 3;
    const unsigned l6 = lane < 48 ? lane : 0, t1o = (cu64 * 32 + pw * 8 + l6 / 6) * 3072u + (l6 % 6) * 128;
    unsigned wol = (b * 4 + pw) * 7 + (lane - 48);
    wol = wol < 6400 ? wol : 6399;
    cptr t1p = lane >= 48 && lane < 55 ? wo + wol * 128u : w1 + (xcd >> 1) * 768 + t1o;
#pragma unroll 1
    for (int s = 0; s < 8; ++s) {
      half(0, true, (s & 3) * STAGE);
      BAR();
      if (s == ts) asm volatile("global_load_dword %0, %1, %2" : "+v"(junk3) : "v"(t2o), "s"(w2 + (xcd >> 1) * 1024) : "memory");
      if (s == ts1) asm volatile("global_load_dword %0, %1, off" : "+v"(junk2) : "v"(t1p) : "memory");
      half(1, true, ((s + 1) & 3) * STAGE);
    }
    half(0, true, 0);
    half(1, false, 0);
    BAR();
    xb[0][4] = lds128(STAGE + xa0 + 8192);
#pragma unroll
    for (int i = 0; i < 4; ++i) af[0][i] = cat(ldstr(STAGE + wa[i]), ldstr(STAGE + wa[i] + 3840));
#pragma unroll
    for (int j = 3; j >= 0; --j) xb[0][j] = lds128(STAGE + xa0 + j * 2048);
    f32x2 mr[5];
#pragma unroll
    for (int j = 0; j < 5; ++j) mr[j] = lds64f(STAT + (j * 16 + r16) * 8);
    SCHED();
    half(0, true, STAGE);
    WAITV(0);
    PIN(junk); PIN(junk1); PIN(junk2); PIN(junk3); PIN(ep);
    BAR();
#pragma unroll
    for (int j = 0; j < 5; ++j) {
      const float m_ = mr[j].x * (1.f / D_), v_ = fmaxf(mr[j].y * (1.f / D_) - m_ * m_, 0.f);
      mr[j] = f32x2{m_, __builtin_amdgcn_rsqf(v_ + EPS)};
    }
#pragma unroll
    for (int j = 0; j < 5; ++j)
#pragma unroll
      for (int i = 0; i < 4; ++i) acc1[i][j] = mfma(af[1][i], xb[1][j], acc1[i][j]);
    const unsigned qdst = QKV + r16 * QP + g * 8;
    f32x4 cs[4];
#pragma unroll
    for (int i = 0; i < 4; ++i)
#pragma unroll
      for (int e = 0; e < 4; ++e) cs[i][e] = dpp<0x150>(acc1[i][0][e]);
#pragma unroll
    for (int j = 0; j < 5; ++j)
#pragma unroll
      for (int i = 0; i < 4; ++i) {
        if (j == 0 && i == 0) continue;
        const f32x4 a = acc1[i][j];
        const float m = mr[j].x, r = mr[j].y;
        sts64(qdst + (pw + 4 * i < 15 ? pw + 4 * i : 14) * 32 + j * 16 * QP,
              v2u{pk((a.x - m * cs[i].x) * r, (a.y - m * cs[i].y) * r), pk((a.z - m * cs[i].z) * r, (a.w - m * cs[i].w) * r)});
      }
    asm volatile(".Lfb_cend:");
#pragma unroll
    for (int c = 0; c < 5; ++c) { asm volatile("" : "=v"(xr[c])); asm volatile("" : "=v"(wov[c])); }
#pragma unroll
    for (int n = 0; n < N1A; ++n) asm volatile("" : "=v"(wr[n >> 2][n & 3]));
  }
  const unsigned tag = __builtin_amdgcn_readfirstlane(ep) + 1u;
  BAR();
  {
    v4u kf[2][2], qf[2];
    v2u vt[2][2][2];
    unsigned kb = ka + 8, kb2 = ka2 + 8;
    PIN(kb); PIN(kb2);
#pragma unroll
    for (int u = 0; u < 2; ++u) {
      qf[u] = cat(lds64(ka - 160 + 16 * QP + u * 40), lds64(kb - 160 + 16 * QP + u * 40));
      SCHED();
      kf[u][0] = cat(lds64(ka + u * 40), lds64(kb + u * 40));
      SCHED();
      kf[u][1] = cat(lds64(ka2 + u * 40), lds64(kb2 + u * 40));
      SCHED();
    }
#pragma unroll
    for (int u = 0; u < 2; ++u) {
      vt[u][0][0] = ldstr(va + u * 40); vt[u][1][0] = ldstr(va2 + u * 40);
      vt[u][0][1] = ldstr(va + u * 40 + 32); vt[u][1][1] = ldstr(va2 + u * 40 + 32);
    }
    SCHED();
    if (w >= 4) issue_wo(2);
    SCHED();
    f32x4 sa[2], sb_[2];
#pragma unroll
    for (int u = 0; u < 2; ++u) {
      const v4u q = v4u{qf[u].x & m3, qf[u].y & m3, qf[u].z & m2, qf[u].w & m2};
      sa[u] = mfma(kf[u][0], q, ba); sb_[u] = mfma(kf[u][1], q, bb);
    }
    constexpr float C = 0.22360679774997896f * 1.4426950408889634f;
    float mc[2];
#pragma unroll
    for (int u = 0; u < 2; ++u)
      mc[u] = mx2(mx2(mx2(sa[u].x, sa[u].y), mx2(sa[u].z, sa[u].w)), mx2(mx2(sb_[u].x, sb_[u].y), mx2(sb_[u].z, sb_[u].w)));
#pragma unroll
    for (int u = 0; u < 2; ++u) mc[u] = rows_max(mc[u]) * C;
    f32x4 o0[2], o1[2], sum[2];
#pragma unroll
    for (int u = 0; u < 2; ++u) {
      float p[8];
#pragma unroll
      for (int r = 0; r < 4; ++r) { p[r] = __builtin_amdgcn_exp2f(fmaf(sa[u][r], C, -mc[u])); p[4 + r] = __builtin_amdgcn_exp2f(fmaf(sb_[u][r], C, -mc[u])); }
      const v4u pf = v4u{pk(p[0], p[1]), pk(p[2], p[3]), pk(p[4], p[5]), pk(p[6], p[7])};
      const f32x4 z = f32x4{0.f, 0.f, 0.f, 0.f};
      sum[u] = mfma(v4u{ONES, ONES, ONES, ONES}, pf, z);
      o0[u] = mfma(cat(vt[u][0][0], vt[u][1][0]), pf, z);
      o1[u] = mfma(cat(vt[u][0][1], vt[u][1][1]), pf, z);
    }
    const float i0 = __builtin_amdgcn_rcpf(sum[0].x), i1 = __builtin_amdgcn_rcpf(sum[1].x);
    const int P = 2 * dt + ahp;
    const unsigned ob_ = OF_OFF + ((st * 4 + 2 * at + apar) * 20 + (P >> 3) * 10) * 1024;
    __builtin_amdgcn_raw_buffer_store_b128(v4u{pk(o0[0].x * i0, o0[0].y * i0), pk(o0[0].z * i0, o0[0].w * i0), pk(o0[1].x * i1, o0[1].y * i1), pk(o0[1].z * i1, o0[1].w * i1)},
                                           rs, lo, ob_ + (P & 7) * 1024, 16);
    if (lane < 16)
      __builtin_amdgcn_raw_buffer_store_b128(v4u{pk(o1[0].x * i0, o1[0].y * i0), pk(o1[0].z * i0, o1[0].w * i0), pk(o1[1].x * i1, o1[1].y * i1), pk(o1[1].z * i1, o1[1].w * i1)},
                                             rs, lo, ob_ + (8 + ((P >> 2) & 1)) * 1024 + (P & 3) * 256, 16);
    WAITV(0);
    asm volatile("s_barrier" ::: "memory");
    if (tid == 0) __builtin_amdgcn_raw_buffer_store_b32(tag, rs, F1_OFF + st * 256 + dt * 4, 0, 16);
  }
  RELANE();

  const int kh = w >> 2, tj = w & 3;
  constexpr unsigned RED = 40960, HIM = 21504, HRI = 147456;
  v4u hres[2];
  {
    cptr ob = ws + OF_OFF + (size_t)((st * 4 + tj) * 20 + kh * 10) * 1024;
    asm volatile("" : "+s"(ob));
    unsigned ov[9];
#pragma unroll
    for (int j = 0; j < 9; ++j) { ov[j] = lo + (j < 8 ? (j + rot2) & 7 : 8) * 1024; PIN(ov[j]); }
    wait_flags(ws, F1_OFF + st * 256 + (kh * 4 + (lane & 3)) * 4, tag);
    v4u of[10];
#pragma unroll
    for (int j = 0; j < 9; ++j) gld16c<0>(of[j], ov[j], ob);
    gld16c<1024>(of[9], ov[8], ob);
    f32x4 acc[5];
#pragma unroll
    for (int c = 0; c < 5; ++c) acc[c] = f32x4{0.f, 0.f, 0.f, 0.f};
    const unsigned wl = ((g >> 1) * 16 + (g & 1) * 4 + q4) * 160 + p4 * 8 + kh * 5120;
    v4u af[2][5];
    auto rda = [&](int j) {
#pragma unroll
      for (int c = 0; c < 5; ++c) {
        const unsigned a = (j < 8 ? 81920 + j * 10240 : (j - 8) * 10240) + wl + c * 32;
        af[j & 1][c] = cat(ldstr(a), ldstr(a + 1280));
      }
    };
    rda(0);
#pragma unroll
    for (int j = 0; j < 10; ++j) {
      if (j < 9) rda(j + 1);
      WAITV(9 - j + n1b(j));
      PIN(of[j]);
#pragma unroll
      for (int c = 0; c < 5; ++c) acc[c] = mfma(af[j & 1][c], of[j], acc[c]);
      SCHED();
#pragma unroll
      for (int n = n1b(j); n < n1b(j + 1); ++n) ldw1(N1A + n);
    }
    v2u h[5];
    if (w < 4) {
#pragma unroll
      for (int c = 0; c < 5; ++c) sts128f(RED + (c * 4 + tj) * 1024 + lo, acc[c]);
      BAR();
#pragma unroll
      for (int n = 0; n < N1A; ++n) ldw1r(n);
#pragma unroll
      for (int c = 0; c < 5; ++c) asm volatile("" : "=v"(h[c]));
    } else {
      __builtin_amdgcn_s_setprio(3);
      BAR();
      float sm = 0.f, sq = 0.f;
#pragma unroll
      for (int c = 0; c < 5; ++c) {
        const f32x4 a = acc[c] + lds128f(RED + (c * 4 + tj) * 1024 + lo);
        h[c] = v2u{pk(a.x + blo(xr[c].x), a.y + bhi(xr[c].x)), pk(a.z + blo(xr[c].y), a.w + bhi(xr[c].y))};
        sm = dot2(h[c].x, ONES, sm); sq = dot2(h[c].x, h[c].x, sq);
        sm = dot2(h[c].y, ONES, sm); sq = dot2(h[c].y, h[c].y, sq);
      }
      sm = rows_sum(sm); sq = rows_sum(sq);
      if (lane < 16) __builtin_amdgcn_raw_buffer_store_b128(v4u{asu(sm), tag, asu(sq), tag}, rs, GR_OFF + ((st * 4 + tj) * 8 + dt) * 256 + lo, 0, 16);
#pragma unroll
      for (int c = 0; c < 5; ++c) sts64(HRI + (32 * (tj >> 1) + 2 * r16 + (tj & 1)) * OP + c * 32 + g * 8, h[c]);
      WAITV(1);
    }
#pragma unroll
    for (int n = N1A + N1B; n < 32; ++n) ldw1(n);
    if (w < 4) WAITV(32 - (N1A + N1B));
#pragma unroll
    for (int kk = 0; kk < X1; ++kk) xp1(kk);
    if (w >= 4) {
      v4u g0, g1;
      {
        const unsigned go = GR_OFF + (st * 4 + tj) * 2048 + lo;
#pragma unroll 1
        for (unsigned spin = 0; spin < SPIN_CAP; ++spin) {
          asm volatile("global_load_dwordx4 %0, %2, %3 sc1\n\tglobal_load_dwordx4 %1, %2, %3 offset:1024 sc1\n\ts_waitcnt vmcnt(0)"
                       : "=&v"(g0), "=&v"(g1) : "v"(go), "s"(ws) : "memory");
          if (__builtin_amdgcn_ballot_w64(g0.y != tag || g0.w != tag || g1.y != tag || g1.w != tag) == 0ull) break;
          __builtin_amdgcn_s_sleep(1);
        }
      }
      const float ts = rows_sum(asf(g0.x) + asf(g1.x)), tq = rows_sum(asf(g0.z) + asf(g1.z));
      const float mean = ts * (1.f / D_), var = fmaxf(tq * (1.f / D_) - mean * mean, 0.f);
      const float r1 = __builtin_amdgcn_rsqf(var + EPS), r2 = __builtin_amdgcn_rsqf(var * r1 * r1 + EPS);
      const float a = r1 * r2, cc = -mean * a;
      const unsigned him = HIM + tj * 2560;
#pragma unroll
      for (int c = 0; c < 5; ++c)
        sts64(him + r16 * 160 + c * 32 + g * 8, v2u{pk(fmaf(blo(h[c].x), a, cc), fmaf(bhi(h[c].x), a, cc)), pk(fmaf(blo(h[c].y), a, cc), fmaf(bhi(h[c].y), a, cc))});
      const unsigned ta = him + ((g & 1) * 8 + q4) * 160 + p4 * 8;
      const unsigned ho = HF_OFF + ((dt * 5) * 64 + st * 2 + (tj >> 1)) * 1024 + ((tj & 1) * 32 + (lane & 31)) * 16;
#pragma unroll
      for (int rnd = 0; rnd < 3; ++rnd) {
        const int c = rnd < 2 ? 2 * rnd + (g >> 1) : 4;
        const v4u v = cat(ldstr(ta + c * 32), ldstr(ta + c * 32 + 640));
        __builtin_amdgcn_raw_buffer_store_b128(v, rs, ho + c * 65536, 0, 16);
      }
      WAITV(0);
      __builtin_amdgcn_s_setprio(0);
    }
    BAR();
    if (tid == 0) __builtin_amdgcn_raw_buffer_store_b32(tag, rs, F2_OFF + (dt * 8 + (st >> 2)) * 256 + (st & 3) * 4, 0, 16);
    {
      const unsigned ha = HRI + (8 * (r16 & 7) + 4 * (r16 >> 3) + 2 * (w & 1) + (lane >> 5)) * OP + ((lane >> 4) & 1) * 16;
      hres[0] = lds128(ha + (w >> 1) * 32);
      hres[1] = lds128(ha + 128);
    }
  }

  v4u vr[6][4];
  const int rot4 = (2 * (idx >> 3) + (idx >> 4) * (idx >> 3 & 1) + (xcd & 1)) % 6;
  unsigned w2v[4];
#define MKW2V() do { _Pragma("unroll") for (int j_ = 0; j_ < 4; ++j_) { w2v[j_] = (32 * (g & 1) + 4 * (g >> 1) + (r16 >> 3) + 8 * j_) * 4096u + (r16 & 7) * 16; PIN(w2v[j_]); } } while (0)
  cptr w2b = w2 + (size_t)w * (192 * 4096) + st * 128;
  auto ldw2 = [&](int n) {
    int kk = (n >> 2) + rot4;
    kk = kk >= 6 ? kk - 6 : kk;
    gld16(vr[n >> 2][n & 3], w2v[n & 3], w2b + ((kk >> 1) * 64 + (kk & 1) * 2) * 4096);
  };
  auto xp2 = [&](int kk) {
#pragma unroll
    for (int j = 0; j < 4; ++j) PIN(vr[kk][j]);
    xpose(vr[kk]);
#pragma unroll
    for (int j = 0; j < 4; ++j) PIN(vr[kk][j]);
  };
  if (p3) {
    unsigned bias[4];
    {
      const int fb = f3 * 64 + 32 * (g & 1) + 4 * (g >> 1) + 2 * (w >> 2);
#pragma unroll
      for (int r = 0; r < 4; ++r) gld4(bias[r], (fb + 8 * r) * 2, b1);
    }
    WAITV(4);
#pragma unroll
    for (int kk = X1; kk < 8; ++kk) xp1(kk);
    cptr hb[4];
    unsigned hv[8];
#pragma unroll
    for (int j = 0; j < 4; ++j) { hb[j] = ws + HF_OFF + (size_t)((d3 * 4 + j) * 64 + w * 8) * 1024; asm volatile("" : "+s"(hb[j])); }
#pragma unroll
    for (int kk = 0; kk < 8; ++kk) { hv[kk] = lo + ((kk + rot3) & 7) * 1024; PIN(hv[kk]); }
    MKW2V();
    { const int ds = (d3 * 64 + ((lane >> 2) & 1) * 63) / 80; wait_flags(ws, F2_OFF + (ds * 8 + w) * 256 + (lane & 3) * 4, tag); }
    __builtin_amdgcn_s_setprio(2);
    RELANE();
    constexpr int HR = 8;
    v4u hf[HR];
    auto ldh = [&](int n) { gld16c<0>(hf[n % HR], hv[n & 7], hb[n >> 3]); };
#pragma unroll
    for (int n = 0; n < HR; ++n) ldh(n);
#pragma unroll
    for (int dj = 0; dj < 4; ++dj) {
      f32x4 acc[4];
#pragma unroll
      for (int i = 0; i < 4; ++i) acc[i] = f32x4{0.f, 0.f, 0.f, 0.f};
#pragma unroll
      for (int kk = 0; kk < 8; ++kk) {
        const int n = dj * 8 + kk;
        WAITV(31 - n < HR - 1 ? 31 - n : HR - 1);
        PIN(hf[n % HR]);
#pragma unroll
        for (int i = 0; i < 4; ++i) acc[i] = mfma(wr[kk][i], hf[n % HR], acc[i]);
        SCHED();
        if (n + HR < 32) ldh(n + HR);
      }
#pragma unroll
      for (int i = 0; i < 4; ++i) sts128f(w * 16384 + (i * 4 + dj) * 1024 + lo, acc[i]);
    }
    __builtin_amdgcn_s_setprio(0);
#pragma unroll
    for (int n = 0; n < N2A; ++n) ldw2(n);
    BAR();
    if (b == 0 && tid == 0) __builtin_amdgcn_raw_buffer_store_b32(tag, rs, EP_OFF, 0, 16);
    {
      const int dj = w & 3, kp = w >> 2;
      const unsigned ra = (2 * kp * 4 + dj) * 1024 + lo;
      f32x4 s0_ = f32x4{0.f, 0.f, 0.f, 0.f}, s1_ = s0_;
#pragma unroll
      for (int v = 0; v < 8; ++v) { s0_ += lds128f(ra + v * 16384); s1_ += lds128f(ra + v * 16384 + 4096); }
#pragma unroll
      for (int r = 0; r < 4; ++r) PIN(bias[r]);
      v4u u;
      u.x = pk(gelu_tanh(s0_.x + blo(bias[0])), gelu_tanh(s0_.y + blo(bias[1])));
      u.y = pk(gelu_tanh(s0_.z + blo(bias[2])), gelu_tanh(s0_.w + blo(bias[3])));
      u.z = pk(gelu_tanh(s1_.x + bhi(bias[0])), gelu_tanh(s1_.y + bhi(bias[1])));
      u.w = pk(gelu_tanh(s1_.z + bhi(bias[2])), gelu_tanh(s1_.w + bhi(bias[3])));
      __builtin_amdgcn_raw_buffer_store_b128(u, rs, UF_OFF + ((d3 * 4 + dj) * 48 + f3 * 2 + kp) * 1024 + lo, 0, 16);
      WAITV(0);
    }
  } else {
    WAITV(0);
    RELANE(); MKW2V();
#pragma unroll
    for (int n = 0; n < N2A; ++n) ldw2(n);
    const unsigned to = (((idx & 1) * 8 + w) * 96 + (lane >> 3)) * 4096u + (lane & 7) * 128;
    const unsigned long long tstart = __builtin_amdgcn_s_memrealtime();
#pragma unroll 1
    for (int i = 0; i < 12; ++i) {
      unsigned t_;
      asm volatile("global_load_dword %0, %1, %2\n\ts_waitcnt vmcnt(0)" : "=&v"(t_) : "v"(to + ((xcd & 1) ? 11 - i : i) * 32768u), "s"(w2 + (xcd >> 1) * 1024) : "memory");
      if (__builtin_amdgcn_s_memrealtime() - tstart > 450) break;
    }
  }

  {
    BAR();
    if (p3 && tid == 0) __builtin_amdgcn_raw_buffer_store_b32(tag, rs, F3_OFF + (d3 * 4 + f3 / 6) * 256 + (f3 % 6) * 4, 0, 16);
#pragma unroll
    for (int n = N2A; n < 24; ++n) ldw2(n);
    const int ej = lane >> 5, ehalf = (lane >> 4) & 1;
    const int etok = 8 * (r16 & 7) + 4 * (r16 >> 3) + 2 * (w & 1) + ej;
    unsigned bq;
    asm volatile("global_load_ushort %0, %1, %2" : "=v"(bq) : "v"((s0 + etok) * 2), "s"(b2) : "memory");
#pragma unroll
    for (int kk = 0; kk < X2; ++kk) {
      if (4 * kk + 4 > N2A) WAITV(w2wait(kk));
      xp2(kk);
    }
    cptr ub[5];
    unsigned uv[6];
#pragma unroll
    for (int c = 0; c < 5; ++c) { ub[c] = ws + UF_OFF + (size_t)((dt * 5 + c) * 48 + w * 6) * 1024; asm volatile("" : "+s"(ub[c])); }
#pragma unroll
    for (int kk = 0; kk < 6; ++kk) { uv[kk] = lo + (kk + rot4 >= 6 ? kk + rot4 - 6 : kk + rot4) * 1024; PIN(uv[kk]); }
    {
      const int l3 = (lane & 3) < 3 ? (lane & 3) : 0;
      wait_flags(ws, F3_OFF + (((dt * 80) / 64 + ((lane >> 2) & 1)) * 4 + (w >> 1)) * 256 + ((w & 1) * 3 + l3) * 4, tag);
    }
    constexpr int UR = 10, UB = 8;
    v4u uf[UR];
    auto ldu = [&](int n) { gld16c<0>(uf[n % UR], uv[n % 6], ub[n / 6]); };
#pragma unroll
    for (int n = 0; n < UB; ++n) ldu(n);
#pragma unroll
    for (int kk = X2; kk < 6; ++kk) xp2(kk);
#pragma unroll
    for (int n = UB; n < UR; ++n) ldu(n);
#pragma unroll
    for (int c = 0; c < 5; ++c) {
      f32x4 acc[4];
#pragma unroll
      for (int j = 0; j < 4; ++j) acc[j] = f32x4{0.f, 0.f, 0.f, 0.f};
#pragma unroll
      for (int kk = 0; kk < 6; ++kk) {
        const int n = c * 6 + kk;
        WAITV(29 - n < UR - 1 ? 29 - n : UR - 1);
        PIN(uf[n % UR]);
#pragma unroll
        for (int j = 0; j < 4; ++j) acc[j] = mfma(uf[n % UR], vr[kk][j], acc[j]);
        SCHED();
        if (n + UR < 30) ldu(n + UR);
      }
#pragma unroll
      for (int j = 0; j < 4; ++j) sts128f(w * 20480 + (c * 4 + j) * 1024 + lo, acc[j]);
    }
    BAR();
    PIN(bq);
    const float bias = blo(bq);
    const __amdgpu_buffer_rsrc_t ro = __builtin_amdgcn_make_buffer_rsrc((void*)out, 0, 2621440, 0x00020000);
#pragma unroll
    for (int pass = 0; pass < 2; ++pass) {
      if (pass == 0 || w < 2) {
        const int P = pass * 8 + w, ct = P >> 1;
        const unsigned ra = (ct * 4 + 2 * (w & 1) + ej) * 1024 + (ehalf * 32 + r16) * 16;
        f32x4 s0_ = f32x4{0.f, 0.f, 0.f, 0.f}, s1_ = s0_;
#pragma unroll
        for (int v = 0; v < 8; ++v) { s0_ += lds128f(ra + v * 20480); s1_ += lds128f(ra + v * 20480 + 256); }
        const v4u hv = hres[pass];
        v4u o;
        o.x = pk(s0_.x + bias + blo(hv.x), s0_.y + bias + bhi(hv.x)); o.y = pk(s0_.z + bias + blo(hv.y), s0_.w + bias + bhi(hv.y));
        o.z = pk(s1_.x + bias + blo(hv.z), s1_.y + bias + bhi(hv.z)); o.w = pk(s1_.z + bias + blo(hv.w), s1_.w + bias + bhi(hv.w));
        __builtin_amdgcn_raw_buffer_store_b128(o, ro, (s0 + etok) * 1280 + dt * 160 + ct * 32 + ehalf * 16, 0, 0);
      }
    }
  }
  asm volatile(".Lfb_end:");
}

extern "C" void kernel_launch(void* const* d_in, const int* in_sizes, int n_in, void* d_out, int out_size, void* d_ws,
                              size_t ws_size, hipStream_t stream) {
  (void)hipFuncSetAttribute((const void*)fused_block, hipFuncAttributeMaxDynamicSharedMemorySize, 163840);
  fused_block<<<256, 512, 163840, stream>>>((cptr)d_in[0], (cptr)d_in[1], (cptr)d_in[2], (cptr)d_in[4], (cptr)d_in[5], (cptr)d_in[6],
                                            (cptr)d_in[7], (unsigned char*)d_out, (unsigned char*)d_ws);
}
```
